```python
import torch

def _softmax(x, axis=-1):
    return torch.softmax(x, dim=axis)
def _gelu(x):
    return torch.nn.functional.gelu(x, approximate="tanh")
def _silu(x):
    return torch.nn.functional.silu(x)
def _sigmoid(x):
    return torch.sigmoid(x)
def _layernorm(x, axis=-1, eps=1e-5):
    m = x.mean(dim=axis, keepdim=True)
    v = x.var(dim=axis, keepdim=True, unbiased=False)
    return (x - m) / torch.sqrt(v + eps)
def _scatter_add(tgt, idx, src, axis=0):
    return tgt.clone().index_add_(axis, idx, src)
def _segment_sum(src, seg, n):
    out = torch.zeros((n,) + src.shape[1:], dtype=src.dtype)
    return out.index_add_(0, seg, src)
def _pad(x, pw):
    flat = tuple(v for pair in reversed(pw) for v in pair)
    return torch.nn.functional.pad(x, flat)

kernel_name = 'gated_mix__sliding_local_dilated__grouped_kv_mqa__prompt'

def setup_inputs():

    _w = (lambda a: (lambda t: t.to(torch.bfloat16) if t.is_floating_point() else t)(torch.as_tensor(a)))
    def _A_setup():
        B, S, D = 1, 2048, 640
        return {
            "x": _w((lambda *s: torch.randn(*s, dtype=torch.bfloat16) * 0.05)(B, S, D)),
            "wqkv": _w((lambda *s: torch.randn(*s, dtype=torch.bfloat16) * 0.8)(D, 3 * D)),
            "wo": _w((lambda *s: torch.randn(*s, dtype=torch.bfloat16) * 0.05)(D, D)),
        }
    _A_in = _A_setup()

    def _B_setup():
        B, S, D, H = 1, 2048, 640, 32
        Dh = D // H
        return {
            "x": _w((lambda *s: torch.randn(*s, dtype=torch.bfloat16) * 0.05)(B, S, D)),
            "wq": _w((lambda *s: torch.randn(*s, dtype=torch.bfloat16) * 1.6)(D, H * Dh)),
            "wk": _w((lambda *s: torch.randn(*s, dtype=torch.bfloat16) * 1.6)(D, Dh)),
            "wv": _w((lambda *s: torch.randn(*s, dtype=torch.bfloat16) * 0.2)(D, Dh)),
            "wo": _w((lambda *s: torch.randn(*s, dtype=torch.bfloat16) * 0.2)(H * Dh, D)),
        }
    _B_in = _B_setup()

    return {
        "A_x": _A_in["x"],
        "A_wqkv": _A_in["wqkv"],
        "A_wo": _A_in["wo"],
        "B_x": _B_in["x"],
        "B_wq": _B_in["wq"],
        "B_wk": _B_in["wk"],
        "B_wv": _B_in["wv"],
        "B_wo": _B_in["wo"],
        "gate_w": _w((lambda *s: torch.randn(*s, dtype=torch.bfloat16) * 0.05)(640) * 16.0),
    }

def reference(A_x, A_wqkv, A_wo, B_x, B_wq, B_wk, B_wv, B_wo, gate_w):
    def _A_ref(x, wqkv, wo):
        B, S, D = x.shape
        H = 32; Dh = D // H
        W, dil = 8, 2
        qkv = (lambda x, *d: x.permute(*d))((x @ wqkv).reshape(B, S, 3, H, Dh), 2, 0, 3, 1, 4)
        q, k, v = qkv[0], qkv[1], qkv[2]
        scale = Dh ** -0.5
        pos = torch.arange(S)
        off = torch.arange(W) * dil
        idx = pos[:, None] - off[None, :]
        mask = idx >= 0
        idx_c = torch.where(mask, idx, 0).reshape(-1)
        kg = k[:, :, idx_c, :].reshape(B, H, S, W, Dh)
        vg = v[:, :, idx_c, :].reshape(B, H, S, W, Dh)
        logits = torch.einsum("bhsd,bhswd->bhsw", q, kg) * scale
        logits = torch.where(mask[None, None, :, :], logits, -1e9)
        attn = _softmax(logits, axis=-1)
        o = torch.einsum("bhsw,bhswd->bhsd", attn, vg)
        return (lambda x, *d: x.permute(*d))(o, 0, 2, 1, 3).reshape(B, S, D) @ wo

    def _B_ref(x, wq, wk, wv, wo):
        B, S, D = x.shape
        H = 32
        Dh = D // H
        q = (lambda x, *d: x.permute(*d))((x @ wq).reshape(B, S, H, Dh), 0, 2, 1, 3)
        k = x @ wk
        v = x @ wv
        scores = torch.einsum("bhqd,bkd->bhqk", q, k) * (Dh ** -0.5)
        p = _softmax(scores, axis=-1)
        o = torch.einsum("bhqk,bkd->bhqd", p, v)
        return (lambda x, *d: x.permute(*d))(o, 0, 2, 1, 3).reshape(B, S, H * Dh) @ wo

    x = A_x
    g = _sigmoid((x * gate_w).sum(-1, keepdims=True))
    a = _A_ref(x, A_wqkv, A_wo)
    b = _B_ref(x, B_wq, B_wk, B_wv, B_wo)
    return g * a + (1 - g) * b
```

```cpp
#include <hip/hip_runtime.h>
#include <hip/hip_bf16.h>
#include <stdint.h>

#define DI __device__ __forceinline__
typedef __attribute__((ext_vector_type(8))) __bf16 bf16x8;
typedef __attribute__((ext_vector_type(4))) __bf16 bf16x4;
typedef __attribute__((ext_vector_type(2))) __bf16 bf16x2;
typedef __attribute__((ext_vector_type(4))) short s16x4;
typedef __attribute__((ext_vector_type(8))) short s16x8;
typedef __attribute__((ext_vector_type(16))) float f32x16;
typedef __attribute__((ext_vector_type(4))) float f32x4;
typedef __attribute__((ext_vector_type(4))) unsigned u32x4;
typedef __attribute__((ext_vector_type(2))) unsigned u32x2;
typedef __attribute__((address_space(3))) s16x4* lds_s16x4_ptr;

constexpr int S_ = 2048, D_ = 640;
constexpr float QSCALE = 0.22360679774997896f * 1.4426950408889634f;

constexpr size_t WS_QP = 0;
constexpr size_t WS_OA = 4u << 20;
constexpr size_t WS_OB = 8u << 20;
constexpr size_t WS_KV = 12u << 20;
constexpr size_t WS_G = 13u << 20;
constexpr size_t WS_KMAX = (13u << 20) + 65536;
constexpr int KV_TILE = 3584;

DI f32x4 mfma16(bf16x8 a, bf16x8 b, f32x4 c) { return __builtin_amdgcn_mfma_f32_16x16x32_bf16(a, b, c, 0, 0, 0); }
DI f32x16 mfma32(bf16x8 a, bf16x8 b, f32x16 c) { return __builtin_amdgcn_mfma_f32_32x32x16_bf16(a, b, c, 0, 0, 0); }
DI f32x16 mfma32k8(s16x4 a, s16x4 b, f32x16 c) { return __builtin_amdgcn_mfma_f32_32x32x8bf16_1k(a, b, c, 0, 0, 0); }
DI s16x4 trread(const char* p) { return __builtin_amdgcn_ds_read_tr16_b64_v4i16((lds_s16x4_ptr)p); }
DI bf16x8 cat8(s16x4 lo, s16x4 hi) { return __builtin_bit_cast(bf16x8, __builtin_shufflevector(lo, hi, 0, 1, 2, 3, 4, 5, 6, 7)); }
DI bf16x4 pack4(float a, float b, float c, float d) { bf16x4 r; r[0] = (__bf16)a; r[1] = (__bf16)b; r[2] = (__bf16)c; r[3] = (__bf16)d; return r; }
DI float bf2f(__bf16 v) { return (float)v; }

DI void dma16_s(const void* sbase, unsigned voff, unsigned lds_dst) {
  unsigned keep;
  asm volatile("s_mov_b32 %0, m0\n\ts_mov_b32 m0, %3\n\ts_nop 0\n\tglobal_load_lds_dwordx4 %1, %2\n\ts_mov_b32 m0, %0"
               : "=&s"(keep) : "v"(voff), "s"(sbase), "s"(lds_dst) : "memory");
}
DI void dma16_v(const void* gsrc, unsigned lds_dst) {
  unsigned keep;
  asm volatile("s_mov_b32 %0, m0\n\ts_mov_b32 m0, %2\n\ts_nop 0\n\tglobal_load_lds_dwordx4 %1, off\n\ts_mov_b32 m0, %0"
               : "=&s"(keep) : "v"(gsrc), "s"(lds_dst) : "memory");
}
#define WAIT_BAR(n) asm volatile("s_waitcnt vmcnt(" #n ") lgkmcnt(0)\n\ts_barrier" ::: "memory")
#define BAR() asm volatile("s_waitcnt lgkmcnt(0)\n\ts_barrier" ::: "memory")

constexpr int K1_STAGE = 40960, K1_XB = 20480;
constexpr int E_STRIDE = 368;
constexpr int E_O = 160 * E_STRIDE;
constexpr int O_STRIDE = 96;
constexpr int E_ZERO = E_O + 160 * O_STRIDE;

DI void k1_kv_block(char* lds, int kvb, int w, int lane, const __bf16* x, const __bf16* wk, const __bf16* wv,
                    const __bf16* gw, char* KVf, float* gout, float* kmax2) {
  const unsigned lbase = (unsigned)(size_t)lds;
  const int tid = w * 64 + lane, l15 = lane & 15, g = lane >> 4;
  for (int p = w; p < 50; p += 4) {
    const char* src = (p < 25) ? (const char*)wk + p * 1024 : (const char*)wv + (p - 25) * 1024;
    dma16_s(src, lane * 16, lbase + p * 1024);
  }
  for (int k = tid; k < 640; k += 256) {
    bf16x4 v; v[0] = gw[k]; v[1] = (__bf16)0.f; v[2] = (__bf16)0.f; v[3] = (__bf16)0.f;
    *(bf16x4*)(lds + 51200 + k * 40) = v;
  }
  const int r0 = kvb * 128 + w * 32;
  f32x4 acc[2][3] = {};
  const int p4 = lane & 3, q4 = l15 >> 2;
  unsigned wa[3];
#pragma unroll
  for (int ni = 0; ni < 3; ++ni) {
    int cg = 4 * ni + p4;
    int off = cg < 5 ? cg * 8 : (cg < 10 ? 25600 + (cg - 5) * 8 : 51200);
    wa[ni] = off + (8 * g + q4) * 40;
  }
  const __bf16* xr0 = x + (size_t)(r0 + l15) * D_ + 8 * g;
  bf16x8 xf[2][10];
#pragma unroll
  for (int half = 0; half < 2; ++half) {
#pragma unroll
    for (int ks = 0; ks < 10; ++ks) {
      xf[0][ks] = *(const bf16x8*)(xr0 + (half * 10 + ks) * 32);
      xf[1][ks] = *(const bf16x8*)(xr0 + 16 * D_ + (half * 10 + ks) * 32);
    }
    if (half == 0) { WAIT_BAR(0); }
#pragma unroll
    for (int ks = 0; ks < 10; ++ks) {
      const int kb = (half * 10 + ks) * 32 * 40;
#pragma unroll
      for (int ni = 0; ni < 3; ++ni) {
        bf16x8 wf = cat8(trread(lds + wa[ni] + kb), trread(lds + wa[ni] + kb + 160));
        acc[0][ni] = mfma16(wf, xf[0][ks], acc[0][ni]);
        acc[1][ni] = mfma16(wf, xf[1][ks], acc[1][ni]);
      }
    }
  }
  char* T = lds + 77824 + w * 3072;
  float nrm[2];
#pragma unroll
  for (int mi = 0; mi < 2; ++mi) {
    float part = 0.f;
#pragma unroll
    for (int ni = 0; ni < 3; ++ni) {
      const int cg = 4 * ni + g;
      bf16x4 v = pack4(acc[mi][ni][0], acc[mi][ni][1], acc[mi][ni][2], acc[mi][ni][3]);
      if (cg < 5) {
        float a = bf2f(v[0]), b = bf2f(v[1]), c = bf2f(v[2]), d = bf2f(v[3]);
        part += a * a + b * b + c * c + d * d;
        *(bf16x4*)(T + (mi * 16 + l15) * 96 + cg * 8) = v;
      } else if (cg < 10) {
        *(bf16x4*)(T + (mi * 16 + l15) * 96 + 48 + (cg - 5) * 8) = v;
      } else if (cg == 10) {
        float z = acc[mi][ni][0];
        gout[r0 + mi * 16 + l15] = 1.f / (1.f + __expf(-z));
      }
    }
    part += __shfl_xor(part, 16); part += __shfl_xor(part, 32);
    nrm[mi] = part;
  }
  float mx = fmaxf(nrm[0], nrm[1]);
#pragma unroll
  for (int o = 1; o < 16; o <<= 1) mx = fmaxf(mx, __shfl_xor(mx, o));
  const int t = kvb * 4 + w;
  if (lane == 0) kmax2[t] = mx;
  asm volatile("s_waitcnt lgkmcnt(0)" ::: "memory");
  __builtin_amdgcn_wave_barrier();
  char* dst = KVf + (size_t)t * KV_TILE;
  const int key = lane & 31, hf = lane >> 5;
  *(u32x4*)(dst + lane * 16) = *(const u32x4*)(T + key * 96 + hf * 16);
  u32x2 k2 = *(const u32x2*)(T + key * 96 + 32);
  if (hf) { k2[0] = 0x3F80u; k2[1] = 0u; }
  *(u32x2*)(dst + 1024 + lane * 8) = k2;
#pragma unroll
  for (int s = 0; s < 2; ++s) {
    s16x8 v;
#pragma unroll
    for (int j = 0; j < 8; ++j) {
      const int kk = 16 * s + 8 * (j >> 2) + 4 * hf + (j & 3);
      short e = *(const short*)(T + kk * 96 + 48 + (key < 20 ? key : 0) * 2);
      v[j] = key < 20 ? e : (key == 20 ? (short)0x3F80 : (short)0);
    }
    *(s16x8*)(dst + 1536 + s * 1024 + lane * 16) = v;
  }
}

__global__ __launch_bounds__(256, 1) void k1_proj(const __bf16* __restrict__ x, const __bf16* __restrict__ wqkv,
                                                  const __bf16* __restrict__ wq, const __bf16* __restrict__ wk,
                                                  const __bf16* __restrict__ wv, const __bf16* __restrict__ gw,
                                                  char* __restrict__ ws) {
  __shared__ __attribute__((aligned(16))) char lds[3 * K1_STAGE];
  const int tid = threadIdx.x, lane = tid & 63, w = __builtin_amdgcn_readfirstlane(tid >> 6);
  const int b = blockIdx.x, xcd = b & 7, idx = b >> 3;
  const int hp = 8 * (xcd & 1) + (idx & 7), rt = 4 * (xcd >> 1) + (idx >> 3);
  if (rt == 15) {
    k1_kv_block(lds, hp, w, lane, x, wk, wv, gw, ws + WS_KV, (float*)(ws + WS_G), (float*)(ws + WS_KMAX));
    return;
  }
  const unsigned lbase = (unsigned)(size_t)lds;
  const int l15 = lane & 15, g = lane >> 4, wm = w >> 1, wn = w & 1;
  const int r0 = rt * 144 - 16;
  unsigned xoff[5]; const char* wsrc[5]; unsigned wstep[5];
#pragma unroll
  for (int ii = 0; ii < 5; ++ii) {
    const int i = w * 5 + ii;
    const int row = 8 * i + (lane >> 3);
    int s = r0 + row; s = s < 0 ? 0 : (s > S_ - 1 ? S_ - 1 : s);
    xoff[ii] = s * 1280 + (((lane & 7) ^ ((row >> 1) & 7)) << 4);
    const int q = 64 * i + lane, k = q / 20, cp = q - k * 20, c = cp ^ (((k >> 3) & 1) << 1);
    const int seg = c / 5, wi = c - seg * 5;
    if (seg < 3) {
      const int colb = seg == 0 ? 640 : (seg == 1 ? 1280 : 0);
      wsrc[ii] = (const char*)(wqkv + k * 1920 + colb + 40 * hp + 8 * wi); wstep[ii] = 64 * 1920 * 2;
    } else {
      wsrc[ii] = (const char*)(wq + k * 640 + 40 * hp + 8 * wi); wstep[ii] = 64 * 640 * 2;
    }
  }
  auto issue = [&](int st, int buf) {
    const char* xb = (const char*)x + st * 128;
    const unsigned lb = lbase + buf * K1_STAGE + w * 5 * 1024;
#pragma unroll
    for (int ii = 0; ii < 5; ++ii) dma16_s(xb, xoff[ii], lb + ii * 1024);
#pragma unroll
    for (int ii = 0; ii < 5; ++ii) { dma16_v(wsrc[ii], lb + K1_XB + ii * 1024); wsrc[ii] += wstep[ii]; }
  };
  issue(0, 0); issue(1, 1);
  const int xo0 = (wm * 80 + l15) * 128 + ((g ^ ((l15 >> 1) & 7)) << 4);
  const int p4 = lane & 3, q4 = l15 >> 2;
  const int wrow = K1_XB + (8 * g + q4) * 320 + 8 * p4 + 160 * wn;
  const int sgn = (g & 1) ? 32 : 0;
  const int wbe = wrow + (wn ? -sgn : sgn);
  const int wbo = wrow + (wn ? sgn : -sgn);
  f32x4 acc[5][5] = {};
  int buf = 0;
#pragma unroll 1
  for (int st = 0; st < 10; ++st) {
    if (st < 9) { WAIT_BAR(10); } else { WAIT_BAR(0); }
    if (st < 8) { int nb = buf + 2; nb = nb >= 3 ? nb - 3 : nb; issue(st + 2, nb); }
    const char* sb = lds + buf * K1_STAGE;
#pragma unroll
    for (int ks = 0; ks < 2; ++ks) {
      bf16x8 xf[5], wf[5];
#pragma unroll
      for (int mi = 0; mi < 5; ++mi) xf[mi] = *(const bf16x8*)(sb + (xo0 ^ (ks * 64)) + mi * 2048);
#pragma unroll
      for (int ni = 0; ni < 5; ++ni) {
        const char* pw = sb + ((ni & 1) ? wbo : wbe) + ni * 32 + ks * 32 * 320;
        wf[ni] = cat8(trread(pw), trread(pw + 4 * 320));
      }
#pragma unroll
      for (int mi = 0; mi < 5; ++mi)
#pragma unroll
        for (int ni = 0; ni < 5; ++ni) acc[mi][ni] = mfma16(wf[ni], xf[mi], acc[mi][ni]);
    }
    buf = buf == 2 ? 0 : buf + 1;
  }
  BAR();
#pragma unroll
  for (int ni = 0; ni < 5; ++ni) {
    const int gi = 4 * (5 * wn + ni) + g;
    const int sg = gi / 5;
    const int off = gi < 30 ? sg * 48 + (gi - sg * 5) * 8 : 288 + (gi - 30) * 8;
    const float sc = gi < 30 ? 1.f : QSCALE;
#pragma unroll
    for (int mi = 0; mi < 5; ++mi) {
      const int row = wm * 80 + mi * 16 + l15;
      *(bf16x4*)(lds + row * E_STRIDE + off) = pack4(acc[mi][ni][0] * sc, acc[mi][ni][1] * sc, acc[mi][ni][2] * sc, acc[mi][ni][3] * sc);
    }
  }
  for (int i = tid; i < 960; i += 256) {
    const int row = i / 6, sg = i - row * 6;
    u32x2 v; v[0] = (sg == 2 || sg == 3) ? 0x3F80u : 0u; v[1] = 0u;
    *(u32x2*)(lds + row * E_STRIDE + sg * 48 + 40) = v;
  }
  if (tid == 0) { u32x4 z = {0u, 0u, 0u, 0u}; *(u32x4*)(lds + E_ZERO) = z; }
  BAR();
  {
    const int hh = w >> 1, par = w & 1;
    const char* Eb = lds + par * E_STRIDE;
    bf16x8 kf[5], qf[5]; s16x4 vt[5][2];
#pragma unroll
    for (int y = 0; y < 5; ++y) {
      const int rowb = (2 * (16 * y + l15)) * E_STRIDE + hh * 48;
      kf[y] = *(const bf16x8*)(Eb + rowb + (g < 3 ? g * 16 : 0));
      qf[y] = *(const bf16x8*)(g < 3 ? Eb + rowb + 192 + g * 16 : lds + E_ZERO);
      const char* vp = Eb + (2 * (16 * y + 4 * g + q4)) * E_STRIDE + 96 + hh * 48 + p4 * 8;
      vt[y][0] = trread(vp); vt[y][1] = trread(vp + 32);
    }
    const f32x4 z4 = {0.f, 0.f, 0.f, 0.f};
#pragma unroll
    for (int y = 0; y < 5; ++y) {
      f32x4 s1 = mfma16(kf[y], qf[y], z4);
      f32x4 s0 = mfma16(kf[y > 0 ? y - 1 : 0], qf[y], z4);
      const bool first = (rt == 0) && (y == 0);
      float mx = -3.0e38f;
      bool m0[4], m1[4];
#pragma unroll
      for (int r = 0; r < 4; ++r) {
        const int j = 4 * g + r, dq = l15 - j;
        m1[r] = (dq >= 0) && (dq <= 7) && !(first && j < 8);
        m0[r] = (y > 0) && (j >= l15 + 9);
        mx = fmaxf(mx, m1[r] ? s1[r] : -3.0e38f);
        mx = fmaxf(mx, m0[r] ? s0[r] : -3.0e38f);
      }
      mx = fmaxf(mx, __shfl_xor(mx, 16)); mx = fmaxf(mx, __shfl_xor(mx, 32));
      const float mc = mx * QSCALE;
      float p0[4], p1[4];
#pragma unroll
      for (int r = 0; r < 4; ++r) {
        p0[r] = m0[r] ? __builtin_amdgcn_exp2f(s0[r] * QSCALE - mc) : 0.f;
        p1[r] = m1[r] ? __builtin_amdgcn_exp2f(s1[r] * QSCALE - mc) : 0.f;
      }
      bf16x8 pf;
#pragma unroll
      for (int r = 0; r < 4; ++r) { pf[r] = (__bf16)p0[r]; pf[4 + r] = (__bf16)p1[r]; }
      f32x4 o0 = mfma16(cat8(vt[y > 0 ? y - 1 : 0][0], vt[y][0]), pf, z4);
      f32x4 o1 = mfma16(cat8(vt[y > 0 ? y - 1 : 0][1], vt[y][1]), pf, z4);
      const float l = __shfl(o1[0], 16 + l15);
      const float inv = 1.f / l;
      char* orow = lds + E_O + (2 * (16 * y + l15) + par) * O_STRIDE + hh * 40;
      *(bf16x4*)(orow + g * 8) = pack4(o0[0] * inv, o0[1] * inv, o0[2] * inv, o0[3] * inv);
      if (g == 0) *(bf16x4*)(orow + 32) = pack4(o1[0] * inv, o1[1] * inv, o1[2] * inv, o1[3] * inv);
    }
  }
  BAR();
  char* oA = ws + WS_OA; char* Qp = ws + WS_QP;
  for (int i = tid; i < 1440; i += 256) {
    const int rr = i / 10, c = i - rr * 10, row = 16 + rr, s = r0 + row;
    if (s < S_) {
      if (c < 5) *(u32x4*)(oA + (size_t)s * 1280 + hp * 80 + c * 16) = *(const u32x4*)(lds + E_O + row * O_STRIDE + c * 16);
      else *(u32x4*)(Qp + (size_t)s * 1280 + hp * 80 + (c - 5) * 16) = *(const u32x4*)(lds + row * E_STRIDE + 288 + (c - 5) * 16);
    }
  }
}

constexpr int K2_STAGE = 8 * KV_TILE;

DI bf16x8 exp_pack(const f32x16& s, int o) {
  bf16x8 p;
#pragma unroll
  for (int j = 0; j < 8; ++j) p[j] = (__bf16)__builtin_amdgcn_exp2f(s[o + j]);
  return p;
}

__global__ __launch_bounds__(512, 2) void k2_attn(char* __restrict__ ws) {
  __shared__ __attribute__((aligned(16))) char lds[3 * K2_STAGE];
  const int tid = threadIdx.x, lane = tid & 63, w = __builtin_amdgcn_readfirstlane(tid >> 6);
  const unsigned lbase = (unsigned)(size_t)lds;
  const char* KVf = ws + WS_KV;
  const int pr = w & 3, kh = w >> 2;
  const int s0 = blockIdx.x * 8 + pr * 2;
  auto issue = [&](int st, int buf) {
    const char* src = KVf + st * K2_STAGE;
    const unsigned lb = lbase + buf * K2_STAGE;
#pragma unroll
    for (int i = 0; i < 4; ++i) { int p = w + 8 * i; p = p > 27 ? 27 : p; dma16_s(src + p * 1024, lane * 16, lb + p * 1024); }
  };
  issue(0, 0); issue(1, 1);
  const int h = lane & 31, hf = lane >> 5;
  const float* kmax2 = (const float*)(ws + WS_KMAX);
  float km = kmax2[lane];
#pragma unroll
  for (int o = 1; o < 64; o <<= 1) km = fmaxf(km, __shfl_xor(km, o));
  bf16x8 q1[2]; s16x4 q2[2];
#pragma unroll
  for (int a = 0; a < 2; ++a) {
    const char* qrow = ws + WS_QP + (size_t)(s0 + a) * 1280 + h * 40;
    u32x2 lo = *(const u32x2*)(qrow + hf * 16), hi = *(const u32x2*)(qrow + hf * 16 + 8), tl = *(const u32x2*)(qrow + 32);
    u32x4 qq = {lo[0], lo[1], hi[0], hi[1]};
    q1[a] = __builtin_bit_cast(bf16x8, qq);
    bf16x4 t4 = __builtin_bit_cast(bf16x4, tl);
    float n2 = 0.f;
#pragma unroll
    for (int j = 0; j < 8; ++j) { float v = bf2f(q1[a][j]); n2 += v * v; }
    if (hf == 0) {
#pragma unroll
      for (int j = 0; j < 4; ++j) { float v = bf2f(t4[j]); n2 += v * v; }
    }
    n2 += __shfl_xor(n2, 32);
    const float off = __builtin_sqrtf(n2 * km) * 1.01f - 60.f;
    unsigned ob = __float_as_uint(off);
    ob = (off > 0.f) ? ((ob + 0xFFFFu) & 0xFFFF0000u) : (ob & 0xFFFF0000u);
    u32x2 t2 = tl;
    if (hf) { t2[0] = (ob >> 16) ^ 0x8000u; t2[1] = 0u; }
    q2[a] = __builtin_bit_cast(s16x4, t2);
  }
  f32x16 oacc[2] = {};
  const f32x16 z16 = {};
  int buf = 0;
#pragma unroll 1
  for (int st = 0; st < 8; ++st) {
    if (st < 7) { WAIT_BAR(4); } else { WAIT_BAR(0); }
    if (st < 6) { int nb = buf + 2; nb = nb >= 3 ? nb - 3 : nb; issue(st + 2, nb); }
    const char* sb = lds + buf * K2_STAGE + kh * 4 * KV_TILE;
#pragma unroll
    for (int i = 0; i < 4; ++i) {
      const char* tb = sb + i * KV_TILE;
      const bf16x8 k1 = *(const bf16x8*)(tb + lane * 16);
      const s16x4 k2 = *(const s16x4*)(tb + 1024 + lane * 8);
      const bf16x8 v0 = *(const bf16x8*)(tb + 1536 + lane * 16);
      const bf16x8 v1 = *(const bf16x8*)(tb + 2560 + lane * 16);
#pragma unroll
      for (int a = 0; a < 2; ++a) {
        f32x16 s = mfma32(k1, q1[a], z16);
        s = mfma32k8(k2, q2[a], s);
        const bf16x8 p0 = exp_pack(s, 0), p1 = exp_pack(s, 8);
        oacc[a] = mfma32(v0, p0, oacc[a]);
        oacc[a] = mfma32(v1, p1, oacc[a]);
      }
    }
    buf = buf == 2 ? 0 : buf + 1;
  }
  BAR();
  {
    float* ex = (float*)(lds + (pr * 2 + (kh ^ 1)) * 3072);
    const f32x16& give = kh ? oacc[0] : oacc[1];
#pragma unroll
    for (int r4 = 0; r4 < 3; ++r4) {
      f32x4 v = {give[4 * r4], give[4 * r4 + 1], give[4 * r4 + 2], give[4 * r4 + 3]};
      *(f32x4*)(ex + r4 * 256 + lane * 4) = v;
    }
  }
  BAR();
  {
    const float* ex = (const float*)(lds + (pr * 2 + kh) * 3072);
    const f32x16& mine = kh ? oacc[1] : oacc[0];
    float o[12];
#pragma unroll
    for (int r4 = 0; r4 < 3; ++r4) {
      f32x4 v = *(const f32x4*)(ex + r4 * 256 + lane * 4);
#pragma unroll
      for (int r = 0; r < 4; ++r) o[4 * r4 + r] = mine[4 * r4 + r] + v[r];
    }
    const int s = s0 + kh;
    float l = __shfl(o[8], 32 + h);
    char* orow = ws + WS_OB + (size_t)s * 1280 + h * 40;
    const bool bad = !(l >= 8.6736174e-19f) || !(l <= 3.0e38f);
    if (__any(bad)) {
      const char* qrow = ws + WS_QP + (size_t)s * 1280 + h * 40;
      float q[20], oo[20];
#pragma unroll
      for (int d = 0; d < 20; ++d) { q[d] = bf2f(*(const __bf16*)(qrow + 2 * d)); oo[d] = 0.f; }
      float m = -3.0e38f, ls = 0.f;
#pragma unroll 1
      for (int kk = 0; kk < S_; ++kk) {
        const char* tb = KVf + (size_t)(kk >> 5) * KV_TILE;
        const int kl = kk & 31;
        float sc = 0.f;
#pragma unroll
        for (int d = 0; d < 16; ++d) sc += q[d] * bf2f(*(const __bf16*)(tb + (kl + 32 * (d >> 3)) * 16 + (d & 7) * 2));
#pragma unroll
        for (int d = 16; d < 20; ++d) sc += q[d] * bf2f(*(const __bf16*)(tb + 1024 + kl * 8 + (d - 16) * 2));
        const float mn = fmaxf(m, sc), al = __builtin_amdgcn_exp2f(m - mn), p = __builtin_amdgcn_exp2f(sc - mn);
        const int sx = kl >> 4, k16 = kl & 15, vh = (k16 >> 2) & 1, j = (k16 & 3) + 4 * (k16 >> 3);
        ls = ls * al + p;
#pragma unroll
        for (int d = 0; d < 20; ++d) oo[d] = oo[d] * al + p * bf2f(*(const __bf16*)(tb + 1536 + sx * 1024 + (d + 32 * vh) * 16 + j * 2));
        m = mn;
      }
      const float inv = 1.f / ls;
      if (hf == 0) {
#pragma unroll
        for (int d4 = 0; d4 < 5; ++d4)
          *(bf16x4*)(orow + d4 * 8) = pack4(oo[4 * d4] * inv, oo[4 * d4 + 1] * inv, oo[4 * d4 + 2] * inv, oo[4 * d4 + 3] * inv);
      }
    } else {
      const float inv = 1.f / l;
      *(bf16x4*)(orow + hf * 8) = pack4(o[0] * inv, o[1] * inv, o[2] * inv, o[3] * inv);
      *(bf16x4*)(orow + 16 + hf * 8) = pack4(o[4] * inv, o[5] * inv, o[6] * inv, o[7] * inv);
      if (hf == 0) *(bf16x4*)(orow + 32) = pack4(o[8] * inv, o[9] * inv, o[10] * inv, o[11] * inv);
    }
  }
}

constexpr int K3_STAGE = 36864, K3_AB = 16384, K3_NBUF = 4;

__global__ __launch_bounds__(256, 1) void k3_out(const __bf16* __restrict__ woA, const __bf16* __restrict__ woB,
                                                 const char* __restrict__ ws, __bf16* __restrict__ out) {
  __shared__ __attribute__((aligned(16))) char lds[K3_NBUF * K3_STAGE];
  const int tid = threadIdx.x, lane = tid & 63, w = __builtin_amdgcn_readfirstlane(tid >> 6);
  const unsigned lbase = (unsigned)(size_t)lds;
  const int b = blockIdx.x, xcd = b & 7, idx = b >> 3;
  const int ct = 4 * (xcd & 1) + (idx & 3), rt = 8 * (xcd >> 1) + (idx >> 2);
  const int mh = w & 1, part = w >> 1;
  const int l15 = lane & 15, g = lane >> 4, p4 = lane & 3, q4 = l15 >> 2;
  const char* Ab = ws + (part ? WS_OB : WS_OA);
  const char* Wb = (const char*)(part ? woB : woA);
  unsigned aoff[4], woff[5];
#pragma unroll
  for (int ii = 0; ii < 4; ++ii) {
    const int row = mh * 32 + 8 * ii + (lane >> 3);
    aoff[ii] = (rt * 64 + row) * 1280 + (((lane & 7) ^ ((row >> 1) & 7)) << 4);
  }
#pragma unroll
  for (int ii = 0; ii < 5; ++ii) {
    const int q = 64 * (mh * 5 + ii) + lane, k = q / 10, c = q - k * 10;
    woff[ii] = k * 1280 + ct * 160 + c * 16;
  }
  auto issue = [&](int st, int buf) {
    const char* ab = Ab + st * 128;
    const char* wb = Wb + st * 64 * 1280;
    const unsigned lb = lbase + buf * K3_STAGE;
#pragma unroll
    for (int ii = 0; ii < 4; ++ii) dma16_s(ab, aoff[ii], lb + part * 8192 + (mh * 4 + ii) * 1024);
#pragma unroll
    for (int ii = 0; ii < 5; ++ii) dma16_s(wb, woff[ii], lb + K3_AB + part * 10240 + (mh * 5 + ii) * 1024);
  };
  issue(0, 0); issue(1, 1); issue(2, 2);
  const int f = (l15 >> 1) & 7;
  const int arow = part * 8192 + (mh * 32 + l15) * 128 + (g & 1) * 8;
  const int ao0 = arow + (((g >> 1) ^ f) << 4);
  const int wo0 = K3_AB + part * 10240 + (4 * g + q4) * 160 + p4 * 8;
  f32x4 acc[2][5] = {};
  int buf = 0;
#pragma unroll 1
  for (int st = 0; st < 10; ++st) {
    if (st < 8) { WAIT_BAR(18); } else if (st == 8) { WAIT_BAR(9); } else { WAIT_BAR(0); }
    if (st < 7) { int nb = buf + 3; nb = nb >= 4 ? nb - 4 : nb; issue(st + 3, nb); }
    const char* sb = lds + buf * K3_STAGE;
#pragma unroll
    for (int ks = 0; ks < 2; ++ks) {
      bf16x8 af[2], wf[5];
#pragma unroll
      for (int mi = 0; mi < 2; ++mi) {
        const s16x4 lo = *(const s16x4*)(sb + (ao0 ^ (ks * 64)) + mi * 2048);
        const s16x4 hi = *(const s16x4*)(sb + (ao0 ^ (ks * 64) ^ 32) + mi * 2048);
        af[mi] = cat8(lo, hi);
      }
#pragma unroll
      for (int ni = 0; ni < 5; ++ni) {
        const char* pw = sb + wo0 + ni * 32 + ks * 32 * 160;
        wf[ni] = cat8(trread(pw), trread(pw + 16 * 160));
      }
#pragma unroll
      for (int mi = 0; mi < 2; ++mi)
#pragma unroll
        for (int ni = 0; ni < 5; ++ni) acc[mi][ni] = mfma16(wf[ni], af[mi], acc[mi][ni]);
    }
    buf = buf == 3 ? 0 : buf + 1;
  }
  BAR();
  const float* gp = (const float*)(ws + WS_G);
  constexpr int F_STRIDE = 336, H_OFF = 64 * F_STRIDE, H_STRIDE = 176;
  float gv[2];
#pragma unroll
  for (int mi = 0; mi < 2; ++mi) gv[mi] = gp[rt * 64 + mh * 32 + mi * 16 + l15];
  if (part == 0) {
#pragma unroll
    for (int mi = 0; mi < 2; ++mi)
#pragma unroll
      for (int ni = 0; ni < 5; ++ni) {
        f32x4 v = acc[mi][ni] * gv[mi];
        *(f32x4*)(lds + (mh * 32 + mi * 16 + l15) * F_STRIDE + (ni * 16 + 4 * g) * 4) = v;
      }
  }
  BAR();
  if (part == 1) {
#pragma unroll
    for (int mi = 0; mi < 2; ++mi)
#pragma unroll
      for (int ni = 0; ni < 5; ++ni) {
        const int row = mh * 32 + mi * 16 + l15;
        f32x4 v = *(const f32x4*)(lds + row * F_STRIDE + (ni * 16 + 4 * g) * 4);
        v += acc[mi][ni] * (1.f - gv[mi]);
        *(bf16x4*)(lds + H_OFF + row * H_STRIDE + (ni * 16 + 4 * g) * 2) = pack4(v[0], v[1], v[2], v[3]);
      }
  }
  BAR();
  for (int i = tid; i < 640; i += 256) {
    const int row = i / 10, c = i - row * 10;
    *(u32x4*)((char*)out + (size_t)(rt * 64 + row) * 1280 + ct * 160 + c * 16) = *(const u32x4*)(lds + H_OFF + row * H_STRIDE + c * 16);
  }
}

extern "C" void kernel_launch(void* const* d_in, const int* in_sizes, int n_in, void* d_out, int out_size, void* d_ws,
                              size_t ws_size, hipStream_t stream) {
  const __bf16* x = (const __bf16*)d_in[0];
  const __bf16* wqkv = (const __bf16*)d_in[1];
  const __bf16* woA = (const __bf16*)d_in[2];
  const __bf16* wq = (const __bf16*)d_in[4];
  const __bf16* wk = (const __bf16*)d_in[5];
  const __bf16* wv = (const __bf16*)d_in[6];
  const __bf16* woB = (const __bf16*)d_in[7];
  const __bf16* gw = (const __bf16*)d_in[8];
  char* ws = (char*)d_ws;
  k1_proj<<<256, 256, 0, stream>>>(x, wqkv, wq, wk, wv, gw, ws);
  k2_attn<<<256, 512, 0, stream>>>(ws);
  k3_out<<<256, 256, 0, stream>>>(woA, woB, ws, (__bf16*)d_out);
}
```

```cpp
#include <hip/hip_runtime.h>
#include <hip/hip_bf16.h>
#include <stdint.h>

#define DI __device__ __forceinline__
typedef __attribute__((ext_vector_type(8))) __bf16 bf16x8;
typedef __attribute__((ext_vector_type(4))) __bf16 bf16x4;
typedef __attribute__((ext_vector_type(4))) short s16x4;
typedef __attribute__((ext_vector_type(8))) short s16x8;
typedef __attribute__((ext_vector_type(16))) float f32x16;
typedef __attribute__((ext_vector_type(4))) float f32x4;
typedef __attribute__((ext_vector_type(4))) unsigned u32x4;
typedef __attribute__((ext_vector_type(2))) unsigned u32x2;
typedef __attribute__((address_space(3))) s16x4* lds_s16x4_ptr;

constexpr int S_ = 2048, D_ = 640;
constexpr float QSCALE = 0.22360679774997896f * 1.4426950408889634f;

constexpr size_t WS_QP = 0;
constexpr size_t WS_QKV = 2048 * 1280;
constexpr size_t WS_OA = 32u << 20;
constexpr size_t WS_OB = 36u << 20;
constexpr size_t WS_KV = 12u << 20;
constexpr size_t WS_G = 13u << 20;
constexpr size_t WS_KMAX = (13u << 20) + 65536;
constexpr int KV_TILE = 3584;

DI f32x4 mfma16(bf16x8 a, bf16x8 b, f32x4 c) { return __builtin_amdgcn_mfma_f32_16x16x32_bf16(a, b, c, 0, 0, 0); }
DI f32x16 mfma32(bf16x8 a, bf16x8 b, f32x16 c) { return __builtin_amdgcn_mfma_f32_32x32x16_bf16(a, b, c, 0, 0, 0); }
DI s16x4 trread(const char* p) { return __builtin_amdgcn_ds_read_tr16_b64_v4i16((lds_s16x4_ptr)p); }
DI bf16x8 cat8(s16x4 lo, s16x4 hi) { return __builtin_bit_cast(bf16x8, __builtin_shufflevector(lo, hi, 0, 1, 2, 3, 4, 5, 6, 7)); }
DI bf16x4 pack4(float a, float b, float c, float d) { bf16x4 r; r[0] = (__bf16)a; r[1] = (__bf16)b; r[2] = (__bf16)c; r[3] = (__bf16)d; return r; }
DI float bf2f(__bf16 v) { return (float)v; }
DI float x16_max(float x) {
  auto s = __builtin_amdgcn_permlane16_swap(__float_as_uint(x), __float_as_uint(x), false, false);
  x = fmaxf(__uint_as_float(s[0]), __uint_as_float(s[1]));
  auto t = __builtin_amdgcn_permlane32_swap(__float_as_uint(x), __float_as_uint(x), false, false);
  return fmaxf(__uint_as_float(t[0]), __uint_as_float(t[1]));
}
DI float x16_sum(float x) {
  auto s = __builtin_amdgcn_permlane16_swap(__float_as_uint(x), __float_as_uint(x), false, false);
  x = __uint_as_float(s[0]) + __uint_as_float(s[1]);
  auto t = __builtin_amdgcn_permlane32_swap(__float_as_uint(x), __float_as_uint(x), false, false);
  return __uint_as_float(t[0]) + __uint_as_float(t[1]);
}
DI float x32_sum(float x) {
  auto t = __builtin_amdgcn_permlane32_swap(__float_as_uint(x), __float_as_uint(x), false, false);
  return __uint_as_float(t[0]) + __uint_as_float(t[1]);
}
template <int CTRL> DI float dppf(float x) { return __builtin_bit_cast(float, __builtin_amdgcn_mov_dpp(__builtin_bit_cast(int, x), CTRL, 0xf, 0xf, true)); }
DI float row16_max(float x) {
  x = fmaxf(x, dppf<0xB1>(x)); x = fmaxf(x, dppf<0x4E>(x)); x = fmaxf(x, dppf<0x141>(x)); x = fmaxf(x, dppf<0x140>(x));
  return x;
}

DI u32x4 pair_pack(const f32x4& a, const f32x4& b) {
  u32x2 pa = __builtin_bit_cast(u32x2, pack4(a[0], a[1], a[2], a[3])), pb = __builtin_bit_cast(u32x2, pack4(b[0], b[1], b[2], b[3]));
  auto s0 = __builtin_amdgcn_permlane16_swap(pa[0], pb[0], false, false);
  auto s1 = __builtin_amdgcn_permlane16_swap(pa[1], pb[1], false, false);
  return u32x4{s0[0], s1[0], s0[1], s1[1]};
}
DI u32x4 quad_pack(const f32x16& a, int q) {
  u32x2 pa = __builtin_bit_cast(u32x2, pack4(a[4 * q], a[4 * q + 1], a[4 * q + 2], a[4 * q + 3]));
  u32x2 pb = __builtin_bit_cast(u32x2, pack4(a[4 * q + 4], a[4 * q + 5], a[4 * q + 6], a[4 * q + 7]));
  auto s0 = __builtin_amdgcn_permlane32_swap(pa[0], pb[0], false, false);
  auto s1 = __builtin_amdgcn_permlane32_swap(pa[1], pb[1], false, false);
  return u32x4{s0[0], s1[0], s0[1], s1[1]};
}
DI void dma16(const void* sbase, unsigned voff, unsigned lds_dst) {
  unsigned keep;
  asm volatile("s_mov_b32 %0, m0\n\ts_mov_b32 m0, %3\n\ts_nop 0\n\tglobal_load_lds_dwordx4 %1, %2\n\ts_mov_b32 m0, %0"
               : "=&s"(keep) : "v"(voff), "s"(sbase), "s"(lds_dst) : "memory");
}
DI void dma4(const void* sbase, unsigned voff, unsigned lds_dst) {
  unsigned keep;
  asm volatile("s_mov_b32 %0, m0\n\ts_mov_b32 m0, %3\n\ts_nop 0\n\tglobal_load_lds_dword %1, %2\n\ts_mov_b32 m0, %0"
               : "=&s"(keep) : "v"(voff), "s"(sbase), "s"(lds_dst) : "memory");
}
#define WAIT_BAR(n) asm volatile("s_waitcnt vmcnt(" #n ") lgkmcnt(0)\n\ts_barrier" ::: "memory")
#define BAR() asm volatile("s_waitcnt lgkmcnt(0)\n\ts_barrier" ::: "memory")
#define RAWBAR() asm volatile("s_barrier" ::: "memory")

constexpr int G_STAGE = 40960, G_NB = 4;
constexpr int K1_XB = 16384;

DI void k1_kv_block(char* lds, int kvb, int w, int lane, const __bf16* x, const __bf16* wk, const __bf16* wv,
                    const __bf16* gw, char* KVf, float* gout, float* kmax2) {
  const unsigned lbase = (unsigned)(size_t)lds;
  const int tid = w * 64 + lane, l15 = lane & 15, g = lane >> 4;
  for (int p = w; p < 50; p += 8) {
    const char* src = (p < 25) ? (const char*)wk + p * 1024 : (const char*)wv + (p - 25) * 1024;
    dma16(src, lane * 16, lbase + p * 1024);
  }
  for (int k = tid; k < 640; k += 512) {
    bf16x4 v; v[0] = gw[k]; v[1] = (__bf16)0.f; v[2] = (__bf16)0.f; v[3] = (__bf16)0.f;
    *(bf16x4*)(lds + 51200 + k * 40) = v;
  }
  if (w >= 4) { WAIT_BAR(0); return; }
  const int r0 = kvb * 128 + w * 32;
  f32x4 acc[2][3] = {};
  const int p4 = lane & 3, q4 = l15 >> 2;
  unsigned wa[3];
#pragma unroll
  for (int ni = 0; ni < 3; ++ni) {
    int cg = 4 * ni + p4;
    int off = cg < 5 ? cg * 8 : (cg < 10 ? 25600 + (cg - 5) * 8 : 51200);
    wa[ni] = off + (8 * g + q4) * 40;
  }
  const __bf16* xr0 = x + (size_t)(r0 + l15) * D_ + 8 * g;
  bf16x8 xf[2][20];
#pragma unroll
  for (int ks = 0; ks < 20; ++ks) {
    xf[0][ks] = *(const bf16x8*)(xr0 + ks * 32);
    xf[1][ks] = *(const bf16x8*)(xr0 + 16 * D_ + ks * 32);
  }
  WAIT_BAR(0);
#pragma unroll
  for (int ks = 0; ks < 20; ++ks) {
    const int kb = ks * 32 * 40;
#pragma unroll
    for (int ni = 0; ni < 3; ++ni) {
      bf16x8 wf = cat8(trread(lds + wa[ni] + kb), trread(lds + wa[ni] + kb + 160));
      acc[0][ni] = mfma16(wf, xf[0][ks], acc[0][ni]);
      acc[1][ni] = mfma16(wf, xf[1][ks], acc[1][ni]);
    }
  }
  char* T = lds + 77824 + w * 3072;
  float nrm[2];
#pragma unroll
  for (int mi = 0; mi < 2; ++mi) {
    float part = 0.f;
#pragma unroll
    for (int ni = 0; ni < 3; ++ni) {
      const int cg = 4 * ni + g;
      bf16x4 v = pack4(acc[mi][ni][0], acc[mi][ni][1], acc[mi][ni][2], acc[mi][ni][3]);
      if (cg < 5) {
        float a = bf2f(v[0]), b = bf2f(v[1]), c = bf2f(v[2]), d = bf2f(v[3]);
        part += a * a + b * b + c * c + d * d;
        *(bf16x4*)(T + (mi * 16 + l15) * 96 + cg * 8) = v;
      } else if (cg < 10) {
        *(bf16x4*)(T + (mi * 16 + l15) * 96 + 48 + (cg - 5) * 8) = v;
      } else if (cg == 10) {
        float z = acc[mi][ni][0];
        gout[r0 + mi * 16 + l15] = 1.f / (1.f + __expf(-z));
      }
    }
    nrm[mi] = x16_sum(part);
  }
  const float mx = row16_max(fmaxf(nrm[0], nrm[1]));
  const int t = kvb * 4 + w;
  if (lane == 0) kmax2[t] = mx;
  asm volatile("s_waitcnt lgkmcnt(0)" ::: "memory");
  __builtin_amdgcn_wave_barrier();
  char* dst = KVf + (size_t)t * KV_TILE;
  const int key = lane & 31, hf = lane >> 5;
  *(u32x4*)(dst + lane * 16) = *(const u32x4*)(T + key * 96 + hf * 16);
  u32x2 k2 = *(const u32x2*)(T + key * 96 + 32);
  if (hf) { k2[0] = 0x3F80u; k2[1] = 0u; }
  *(u32x2*)(dst + 1024 + lane * 8) = k2;
#pragma unroll
  for (int s = 0; s < 2; ++s) {
    s16x8 v;
#pragma unroll
    for (int j = 0; j < 8; ++j) {
      const int kk = 16 * s + 8 * (j >> 2) + 4 * hf + (j & 3);
      short e = *(const short*)(T + kk * 96 + 48 + (key < 20 ? key : 0) * 2);
      v[j] = key < 20 ? e : (key == 20 ? (short)0x3F80 : (short)0);
    }
    *(s16x8*)(dst + 1536 + s * 1024 + lane * 16) = v;
  }
}

__global__ __launch_bounds__(512, 2) void k1_proj(const __bf16* __restrict__ x, const __bf16* __restrict__ wqkv,
                                                  const __bf16* __restrict__ wq, const __bf16* __restrict__ wk,
                                                  const __bf16* __restrict__ wv, const __bf16* __restrict__ gw,
                                                  char* __restrict__ ws) {
  __shared__ __attribute__((aligned(16))) char lds[G_NB * G_STAGE];
  const int tid = threadIdx.x, lane = tid & 63, w = __builtin_amdgcn_readfirstlane(tid >> 6);
  const int b = blockIdx.x, xcd = b & 7, idx = b >> 3;
  if (idx >= 28) {
    k1_kv_block(lds, xcd * 2 + (idx - 28), w, lane, x, wk, wv, gw, ws + WS_KV, (float*)(ws + WS_G), (float*)(ws + WS_KMAX));
    return;
  }
  const int rt = 4 * (xcd >> 1) + idx / 7, ct = 7 * (xcd & 1) + idx % 7;
  const unsigned lbase = (unsigned)(size_t)lds;
  const int l15 = lane & 15, g = lane >> 4;
  if (w >= 4) {
    const int lw = w - 4;
    const bool isq = ct >= 10;
    const char* wb = isq ? (const char*)wq : (const char*)wqkv;
    const unsigned wstride = isq ? 1280u : 3840u;
    const int col0 = isq ? (ct - 10) * 192 : ct * 192;
    const int rX = idx % 7, rW = (0x5320 >> (4 * (idx / 7))) & 15;
    unsigned xoff[4], woff[6], xl[4], wl[6];
#pragma unroll
    for (int ii = 0; ii < 4; ++ii) {
      const int p = (lw * 4 + ii + rX) & 15;
      const int row = 8 * p + (lane >> 3);
      xoff[ii] = (rt * 128 + row) * 1280 + (((lane & 7) ^ ((row >> 1) & 7)) << 4);
      xl[ii] = p * 1024;
    }
#pragma unroll
    for (int ii = 0; ii < 6; ++ii) {
      int p = lw * 6 + ii + rW; p = p >= 24 ? p - 24 : p;
      const int q = 64 * p + lane, k = q / 24, cp = q - k * 24;
      int col = col0 + (((cp >> 1) ^ (k & 2)) << 4) + ((cp & 1) << 3);
      if (isq && col > 632) col = 632;
      woff[ii] = k * wstride + col * 2;
      wl[ii] = K1_XB + p * 1024;
    }
    auto issue = [&](int st, int buf) {
      const char* xb = (const char*)x + st * 128;
      const char* wp = wb + (size_t)st * 64 * wstride;
      const unsigned lb = lbase + buf * G_STAGE;
      dma16(xb, xoff[0], lb + xl[0]); dma16(wp, woff[0], lb + wl[0]); dma16(wp, woff[1], lb + wl[1]);
      dma16(xb, xoff[1], lb + xl[1]); dma16(wp, woff[2], lb + wl[2]); dma16(xb, xoff[2], lb + xl[2]);
      dma16(wp, woff[3], lb + wl[3]); dma16(wp, woff[4], lb + wl[4]); dma16(xb, xoff[3], lb + xl[3]);
      dma16(wp, woff[5], lb + wl[5]);
    };
    issue(0, 0); issue(1, 1); issue(2, 2);
    WAIT_BAR(10);
#pragma unroll 1
    for (int st = 0; st < 9; ++st) {
      if (st < 7) { issue(st + 3, (st + 3) & 3); WAIT_BAR(10); }
      else { WAIT_BAR(0); }
    }
  } else {
    const int wm = w >> 1, wn = w & 1;
    const int l31 = lane & 31, h = lane >> 5, p4 = lane & 3, q4 = l15 >> 2;
    const int xo0 = (wm * 64 + l31) * 128 + ((h ^ ((l31 >> 1) & 7)) << 4);
    const int wo0 = K1_XB + (8 * h + q4) * 384 + 8 * p4 + 32 * ((6 * wn + (g & 1)) ^ (q4 & 2));
    const int wo1 = K1_XB + (8 * h + q4) * 384 + 8 * p4 + 32 * ((6 * wn + 2 + (g & 1)) ^ (q4 & 2));
    f32x16 acc[2][3] = {};
    bf16x8 xa[2], wa[3], xb[2], wc[3];
    auto ldf = [&](bf16x8 (&xf)[2], bf16x8 (&wf)[3], const char* sb, int ks) {
#pragma unroll
      for (int mi = 0; mi < 2; ++mi) xf[mi] = *(const bf16x8*)(sb + (xo0 ^ (ks * 32)) + mi * 4096);
#pragma unroll
      for (int ni = 0; ni < 3; ++ni) {
        const char* pw = sb + (ni == 1 ? wo1 : wo0) + (ni == 2 ? 128 : 0) + ks * 16 * 384;
        wf[ni] = cat8(trread(pw), trread(pw + 4 * 384));
      }
    };
    auto mm = [&](const bf16x8 (&xf)[2], const bf16x8 (&wf)[3]) {
#pragma unroll
      for (int mi = 0; mi < 2; ++mi)
#pragma unroll
        for (int ni = 0; ni < 3; ++ni) acc[mi][ni] = mfma32(wf[ni], xf[mi], acc[mi][ni]);
#pragma unroll
      for (int i = 0; i < 4; ++i) { __builtin_amdgcn_sched_group_barrier(0x8, 1, 0); __builtin_amdgcn_sched_group_barrier(0x100, 2, 0); }
      __builtin_amdgcn_sched_group_barrier(0x8, 2, 0);
      __builtin_amdgcn_sched_barrier(0);
    };
    WAIT_BAR(10);
    ldf(xa, wa, lds, 0);
#pragma unroll
    for (int st = 0; st < 10; ++st) {
      const char* sb = lds + (st & 3) * G_STAGE;
      const char* sn = lds + ((st + 1) & 3) * G_STAGE;
      ldf(xb, wc, sb, 1); mm(xa, wa);
      ldf(xa, wa, sb, 2); mm(xb, wc);
      ldf(xb, wc, sb, 3); mm(xa, wa);
      if (st < 9) ldf(xa, wa, sn, 0);
      mm(xb, wc);
      if (st < 9) RAWBAR();
    }
    char* dst; int npan, pan0;
    if (ct >= 10) {
      dst = ws + WS_QP; npan = 40; pan0 = (ct - 10) * 12;
#pragma unroll
      for (int mi = 0; mi < 2; ++mi)
#pragma unroll
        for (int ni = 0; ni < 3; ++ni) acc[mi][ni] *= QSCALE;
    } else { dst = ws + WS_QKV; npan = 120; pan0 = ct * 12; }
    pan0 += wn * 6;
    dst += (size_t)((rt * 4 + wm * 2) * npan) * 1024 + l31 * 32 + h * 16;
#pragma unroll
    for (int mi = 0; mi < 2; ++mi)
#pragma unroll
      for (int ni = 0; ni < 3; ++ni)
#pragma unroll
        for (int qq = 0; qq < 2; ++qq) {
          const u32x4 v = quad_pack(acc[mi][ni], 2 * qq);
          const int pan = pan0 + 2 * ni + qq;
          if (pan < npan) *(u32x4*)(dst + (size_t)(mi * npan + pan) * 1024) = v;
        }
  }
}

constexpr int K2_STAGE = 8 * KV_TILE;
constexpr int K2_RA = 3 * K2_STAGE;
constexpr int K2_SCR = K2_RA + 40960;

DI void load_q(const char* ws, int s, int lane, float km, bf16x8& q1, s16x4& q2) {
  const int h = lane & 31, hf = lane >> 5;
  const char* qb = ws + WS_QP + (size_t)(s >> 5) * 40 * 1024 + (s & 31) * 32;
  auto qa = [&](int col) { return *(const u32x2*)(qb + (col >> 4) * 1024 + (col & 15) * 2); };
  const u32x2 lo = qa(20 * h + 8 * hf), hi = qa(20 * h + 8 * hf + 4), tl = qa(20 * h + 16);
  u32x4 qq = {lo[0], lo[1], hi[0], hi[1]};
  q1 = __builtin_bit_cast(bf16x8, qq);
  bf16x4 t4 = __builtin_bit_cast(bf16x4, tl);
  float n2 = 0.f;
#pragma unroll
  for (int j = 0; j < 8; ++j) { float v = bf2f(q1[j]); n2 += v * v; }
  float n3 = 0.f;
#pragma unroll
  for (int j = 0; j < 4; ++j) { float v = bf2f(t4[j]); n3 += v * v; }
  n2 = x32_sum(n2) + n3;
  const float off = __builtin_sqrtf(n2 * km) * 1.01f - 60.f;
  unsigned ob = __float_as_uint(off);
  ob = (off > 0.f) ? ((ob + 0xFFFFu) & 0xFFFF0000u) : (ob & 0xFFFF0000u);
  u32x2 t2 = tl;
  if (hf) { t2[0] = (ob >> 16) ^ 0x8000u; t2[1] = 0u; }
  q2 = __builtin_bit_cast(s16x4, t2);
}

__global__ __launch_bounds__(512, 2) void k2_attn(char* __restrict__ ws, const char* __restrict__ woA, const char* __restrict__ woB) {
  __shared__ __attribute__((aligned(16))) char lds[K2_SCR + 256];
  const int tid = threadIdx.x, lane = tid & 63, w = __builtin_amdgcn_readfirstlane(tid >> 6);
  const unsigned lbase = (unsigned)(size_t)lds;
  const char* KVf = ws + WS_KV;
  const int pr = w & 3, kh = w >> 2;
  const int s0 = blockIdx.x * 8 + pr * 2;
  const int wq = w > 6 ? 6 : w;
  unsigned dv = lane * 16 + wq * 4096;
  {
    const unsigned ld = lbase + wq * 4096;
#pragma unroll
    for (int st = 0; st < 2; ++st)
#pragma unroll
      for (int k = 0; k < 4; ++k) dma16(KVf + st * K2_STAGE + k * 1024, dv, ld + st * K2_STAGE + k * 1024);
    dv += 2 * K2_STAGE;
  }
  float km = ((const float*)(ws + WS_KMAX))[lane];
  bf16x8 q1a, q1b; s16x4 q2a, q2b;
  km = x16_max(row16_max(km));
  load_q(ws, s0, lane, km, q1a, q2a); load_q(ws, s0 + 1, lane, km, q1b, q2b);
  f32x16 oa = {}, ob = {};
  WAIT_BAR(0);
  const int hp = blockIdx.x & 15, art = blockIdx.x >> 4;
  unsigned rav, rald = lbase + K2_RA + w * 1024;
  const char* rap = ws + WS_QKV + ((long)art * 4 - 1) * 120 * 1024;
  {
    int c = (lane & 15) ^ ((2 * w + (lane >> 5)) & 15); c = c > 14 ? 14 : c;
    const int seg = c / 5, wi = c - seg * 5;
    const int col = seg * 640 + hp * 40 + wi * 8;
    const int r = 16 + 4 * w + (lane >> 4);
    rav = ((r >> 5) * 120 + (col >> 4)) * 1024 + (r & 31) * 32 + (col & 15) * 2;
  }
  {
    u32x4 k1, v0, v1 = {0u, 0u, 0u, 0u}; u32x2 k2;
    unsigned a16, a8, n16, n8;
    const unsigned base16 = lbase + kh * 4 * KV_TILE + lane * 16, base8 = lbase + kh * 4 * KV_TILE + lane * 8;
    unsigned b0 = 0, b1 = K2_STAGE, b2 = 2 * K2_STAGE, st, tmp, keep;
    const unsigned ldw = lbase + wq * 4096;
    asm volatile(
      "s_mov_b32 %[keep], m0\n"
      "v_add_u32 %[a16], %[b0], %[base16]\n"
      "v_add_u32 %[a8], %[b0], %[base8]\n"
      "ds_read_b128 %[k1], %[a16]\n"
      "ds_read_b64 %[k2], %[a8] offset:1024\n"
      "ds_read_b128 %[v0], %[a16] offset:1536\n"
      "v_mov_b32 v100, 0\n"
      "v_mov_b32 v101, 0\n"
      "v_mov_b32 v102, 0\n"
      "v_mov_b32 v103, 0\n"
      "s_waitcnt lgkmcnt(1)\n"
      "v_mfma_f32_32x32x16_bf16 v[64:79], %[k1], %[q1a], 0\n"
      "v_mfma_f32_32x32x8_bf16 v[64:79], %[k2], %[q2a], v[64:79]\n"
      "s_mov_b32 %[st], 0\n"
      "s_nop 7\n"
      "s_nop 7\n"
      "s_branch 2f\n"
      "1:\n"
      "s_waitcnt vmcnt(0)\n"
      "s_barrier\n"
      "2:\n"
      "s_cmp_gt_u32 %[st], 5\n"
      "s_cbranch_scc1 3f\n"
      "s_add_u32 m0, %[b2], %[ldw]\n"
      "s_nop 0\n"
      "global_load_lds_dwordx4 %[dv], %[gp] offset:0\n"
      "global_load_lds_dwordx4 %[dv], %[gp] offset:1024\n"
      "global_load_lds_dwordx4 %[dv], %[gp] offset:2048\n"
      "global_load_lds_dwordx4 %[dv], %[gp] offset:3072\n"
      "v_add_u32 %[dv], 28672, %[dv]\n"
      "3:\n"
      "v_add_u32 %[a16], %[b0], %[base16]\n"
      "v_add_u32 %[a8], %[b0], %[base8]\n"
      "v_add_u32 %[n16], %[b1], %[base16]\n"
      "v_add_u32 %[n8], %[b1], %[base8]\n"
      "v_exp_f32 v104, v64\n"
      "v_exp_f32 v105, v65\n"
      "v_mfma_f32_32x32x16_bf16 %[ob], %[v1], v[100:103], %[ob]\n"
      "ds_read_b128 %[v1], %[a16] offset:2560\n"
      "v_exp_f32 v106, v66\n"
      "v_exp_f32 v107, v67\n"
      "v_cvt_pk_bf16_f32 v96, v104, v105\n"
      "v_cvt_pk_bf16_f32 v97, v106, v107\n"
      "v_exp_f32 v108, v68\n"
      "v_exp_f32 v109, v69\n"
      "v_mfma_f32_32x32x16_bf16 v[80:95], %[k1], %[q1b], 0\n"
      "v_exp_f32 v110, v70\n"
      "v_exp_f32 v111, v71\n"
      "v_cvt_pk_bf16_f32 v98, v108, v109\n"
      "v_cvt_pk_bf16_f32 v99, v110, v111\n"
      "v_exp_f32 v104, v72\n"
      "v_exp_f32 v105, v73\n"
      "v_mfma_f32_32x32x8_bf16 v[80:95], %[k2], %[q2b], v[80:95]\n"
      "ds_read_b128 %[k1], %[a16] offset:3584\n"
      "ds_read_b64 %[k2], %[a8] offset:4608\n"
      "v_exp_f32 v106, v74\n"
      "v_exp_f32 v107, v75\n"
      "v_cvt_pk_bf16_f32 v100, v104, v105\n"
      "v_cvt_pk_bf16_f32 v101, v106, v107\n"
      "v_exp_f32 v108, v76\n"
      "v_exp_f32 v109, v77\n"
      "s_waitcnt lgkmcnt(3)\n"
      "v_mfma_f32_32x32x16_bf16 %[oa], %[v0], v[96:99], %[oa]\n"
      "v_exp_f32 v110, v78\n"
      "v_exp_f32 v111, v79\n"
      "v_cvt_pk_bf16_f32 v102, v108, v109\n"
      "v_cvt_pk_bf16_f32 v103, v110, v111\n"
      "v_exp_f32 v104, v80\n"
      "v_exp_f32 v105, v81\n"
      "s_waitcnt lgkmcnt(2)\n"
      "v_mfma_f32_32x32x16_bf16 %[oa], %[v1], v[100:103], %[oa]\n"
      "v_exp_f32 v106, v82\n"
      "v_exp_f32 v107, v83\n"
      "v_cvt_pk_bf16_f32 v96, v104, v105\n"
      "v_cvt_pk_bf16_f32 v97, v106, v107\n"
      "v_exp_f32 v108, v84\n"
      "v_exp_f32 v109, v85\n"
      "s_waitcnt lgkmcnt(1)\n"
      "v_mfma_f32_32x32x16_bf16 v[64:79], %[k1], %[q1a], 0\n"
      "v_exp_f32 v110, v86\n"
      "v_exp_f32 v111, v87\n"
      "v_cvt_pk_bf16_f32 v98, v108, v109\n"
      "v_cvt_pk_bf16_f32 v99, v110, v111\n"
      "v_exp_f32 v104, v88\n"
      "v_exp_f32 v105, v89\n"
      "s_waitcnt lgkmcnt(0)\n"
      "v_mfma_f32_32x32x8_bf16 v[64:79], %[k2], %[q2a], v[64:79]\n"
      "v_exp_f32 v106, v90\n"
      "v_exp_f32 v107, v91\n"
      "v_cvt_pk_bf16_f32 v100, v104, v105\n"
      "v_cvt_pk_bf16_f32 v101, v106, v107\n"
      "v_exp_f32 v108, v92\n"
      "v_exp_f32 v109, v93\n"
      "v_mfma_f32_32x32x16_bf16 %[ob], %[v0], v[96:99], %[ob]\n"
      "ds_read_b128 %[v0], %[a16] offset:5120\n"
      "v_exp_f32 v110, v94\n"
      "v_exp_f32 v111, v95\n"
      "v_cvt_pk_bf16_f32 v102, v108, v109\n"
      "v_cvt_pk_bf16_f32 v103, v110, v111\n"
      "v_exp_f32 v104, v64\n"
      "v_exp_f32 v105, v65\n"
      "v_mfma_f32_32x32x16_bf16 %[ob], %[v1], v[100:103], %[ob]\n"
      "ds_read_b128 %[v1], %[a16] offset:6144\n"
      "v_exp_f32 v106, v66\n"
      "v_exp_f32 v107, v67\n"
      "v_cvt_pk_bf16_f32 v96, v104, v105\n"
      "v_cvt_pk_bf16_f32 v97, v106, v107\n"
      "v_exp_f32 v108, v68\n"
      "v_exp_f32 v109, v69\n"
      "v_mfma_f32_32x32x16_bf16 v[80:95], %[k1], %[q1b], 0\n"
      "v_exp_f32 v110, v70\n"
      "v_exp_f32 v111, v71\n"
      "v_cvt_pk_bf16_f32 v98, v108, v109\n"
      "v_cvt_pk_bf16_f32 v99, v110, v111\n"
      "v_exp_f32 v104, v72\n"
      "v_exp_f32 v105, v73\n"
      "v_mfma_f32_32x32x8_bf16 v[80:95], %[k2], %[q2b], v[80:95]\n"
      "ds_read_b128 %[k1], %[a16] offset:7168\n"
      "ds_read_b64 %[k2], %[a8] offset:8192\n"
      "v_exp_f32 v106, v74\n"
      "v_exp_f32 v107, v75\n"
      "v_cvt_pk_bf16_f32 v100, v104, v105\n"
      "v_cvt_pk_bf16_f32 v101, v106, v107\n"
      "v_exp_f32 v108, v76\n"
      "v_exp_f32 v109, v77\n"
      "s_waitcnt lgkmcnt(3)\n"
      "v_mfma_f32_32x32x16_bf16 %[oa], %[v0], v[96:99], %[oa]\n"
      "v_exp_f32 v110, v78\n"
      "v_exp_f32 v111, v79\n"
      "v_cvt_pk_bf16_f32 v102, v108, v109\n"
      "v_cvt_pk_bf16_f32 v103, v110, v111\n"
      "v_exp_f32 v104, v80\n"
      "v_exp_f32 v105, v81\n"
      "s_waitcnt lgkmcnt(2)\n"
      "v_mfma_f32_32x32x16_bf16 %[oa], %[v1], v[100:103], %[oa]\n"
      "v_exp_f32 v106, v82\n"
      "v_exp_f32 v107, v83\n"
      "v_cvt_pk_bf16_f32 v96, v104, v105\n"
      "v_cvt_pk_bf16_f32 v97, v106, v107\n"
      "v_exp_f32 v108, v84\n"
      "v_exp_f32 v109, v85\n"
      "s_waitcnt lgkmcnt(1)\n"
      "v_mfma_f32_32x32x16_bf16 v[64:79], %[k1], %[q1a], 0\n"
      "v_exp_f32 v110, v86\n"
      "v_exp_f32 v111, v87\n"
      "v_cvt_pk_bf16_f32 v98, v108, v109\n"
      "v_cvt_pk_bf16_f32 v99, v110, v111\n"
      "v_exp_f32 v104, v88\n"
      "v_exp_f32 v105, v89\n"
      "s_waitcnt lgkmcnt(0)\n"
      "v_mfma_f32_32x32x8_bf16 v[64:79], %[k2], %[q2a], v[64:79]\n"
      "v_exp_f32 v106, v90\n"
      "v_exp_f32 v107, v91\n"
      "v_cvt_pk_bf16_f32 v100, v104, v105\n"
      "v_cvt_pk_bf16_f32 v101, v106, v107\n"
      "v_exp_f32 v108, v92\n"
      "v_exp_f32 v109, v93\n"
      "v_mfma_f32_32x32x16_bf16 %[ob], %[v0], v[96:99], %[ob]\n"
      "ds_read_b128 %[v0], %[a16] offset:8704\n"
      "v_exp_f32 v110, v94\n"
      "v_exp_f32 v111, v95\n"
      "v_cvt_pk_bf16_f32 v102, v108, v109\n"
      "v_cvt_pk_bf16_f32 v103, v110, v111\n"
      "v_exp_f32 v104, v64\n"
      "v_exp_f32 v105, v65\n"
      "v_mfma_f32_32x32x16_bf16 %[ob], %[v1], v[100:103], %[ob]\n"
      "ds_read_b128 %[v1], %[a16] offset:9728\n"
      "v_exp_f32 v106, v66\n"
      "v_exp_f32 v107, v67\n"
      "v_cvt_pk_bf16_f32 v96, v104, v105\n"
      "v_cvt_pk_bf16_f32 v97, v106, v107\n"
      "v_exp_f32 v108, v68\n"
      "v_exp_f32 v109, v69\n"
      "v_mfma_f32_32x32x16_bf16 v[80:95], %[k1], %[q1b], 0\n"
      "v_exp_f32 v110, v70\n"
      "v_exp_f32 v111, v71\n"
      "v_cvt_pk_bf16_f32 v98, v108, v109\n"
      "v_cvt_pk_bf16_f32 v99, v110, v111\n"
      "v_exp_f32 v104, v72\n"
      "v_exp_f32 v105, v73\n"
      "v_mfma_f32_32x32x8_bf16 v[80:95], %[k2], %[q2b], v[80:95]\n"
      "ds_read_b128 %[k1], %[a16] offset:10752\n"
      "ds_read_b64 %[k2], %[a8] offset:11776\n"
      "v_exp_f32 v106, v74\n"
      "v_exp_f32 v107, v75\n"
      "v_cvt_pk_bf16_f32 v100, v104, v105\n"
      "v_cvt_pk_bf16_f32 v101, v106, v107\n"
      "v_exp_f32 v108, v76\n"
      "v_exp_f32 v109, v77\n"
      "s_waitcnt lgkmcnt(3)\n"
      "v_mfma_f32_32x32x16_bf16 %[oa], %[v0], v[96:99], %[oa]\n"
      "v_exp_f32 v110, v78\n"
      "v_exp_f32 v111, v79\n"
      "v_cvt_pk_bf16_f32 v102, v108, v109\n"
      "v_cvt_pk_bf16_f32 v103, v110, v111\n"
      "v_exp_f32 v104, v80\n"
      "v_exp_f32 v105, v81\n"
      "s_waitcnt lgkmcnt(2)\n"
      "v_mfma_f32_32x32x16_bf16 %[oa], %[v1], v[100:103], %[oa]\n"
      "v_exp_f32 v106, v82\n"
      "v_exp_f32 v107, v83\n"
      "v_cvt_pk_bf16_f32 v96, v104, v105\n"
      "v_cvt_pk_bf16_f32 v97, v106, v107\n"
      "v_exp_f32 v108, v84\n"
      "v_exp_f32 v109, v85\n"
      "s_waitcnt lgkmcnt(1)\n"
      "v_mfma_f32_32x32x16_bf16 v[64:79], %[k1], %[q1a], 0\n"
      "v_exp_f32 v110, v86\n"
      "v_exp_f32 v111, v87\n"
      "v_cvt_pk_bf16_f32 v98, v108, v109\n"
      "v_cvt_pk_bf16_f32 v99, v110, v111\n"
      "v_exp_f32 v104, v88\n"
      "v_exp_f32 v105, v89\n"
      "s_waitcnt lgkmcnt(0)\n"
      "v_mfma_f32_32x32x8_bf16 v[64:79], %[k2], %[q2a], v[64:79]\n"
      "v_exp_f32 v106, v90\n"
      "v_exp_f32 v107, v91\n"
      "v_cvt_pk_bf16_f32 v100, v104, v105\n"
      "v_cvt_pk_bf16_f32 v101, v106, v107\n"
      "v_exp_f32 v108, v92\n"
      "v_exp_f32 v109, v93\n"
      "v_mfma_f32_32x32x16_bf16 %[ob], %[v0], v[96:99], %[ob]\n"
      "ds_read_b128 %[v0], %[a16] offset:12288\n"
      "v_exp_f32 v110, v94\n"
      "v_exp_f32 v111, v95\n"
      "v_cvt_pk_bf16_f32 v102, v108, v109\n"
      "v_cvt_pk_bf16_f32 v103, v110, v111\n"
      "v_exp_f32 v104, v64\n"
      "v_exp_f32 v105, v65\n"
      "v_mfma_f32_32x32x16_bf16 %[ob], %[v1], v[100:103], %[ob]\n"
      "ds_read_b128 %[v1], %[a16] offset:13312\n"
      "v_exp_f32 v106, v66\n"
      "v_exp_f32 v107, v67\n"
      "v_cvt_pk_bf16_f32 v96, v104, v105\n"
      "v_cvt_pk_bf16_f32 v97, v106, v107\n"
      "v_exp_f32 v108, v68\n"
      "v_exp_f32 v109, v69\n"
      "v_mfma_f32_32x32x16_bf16 v[80:95], %[k1], %[q1b], 0\n"
      "v_exp_f32 v110, v70\n"
      "v_exp_f32 v111, v71\n"
      "v_cvt_pk_bf16_f32 v98, v108, v109\n"
      "v_cvt_pk_bf16_f32 v99, v110, v111\n"
      "v_exp_f32 v104, v72\n"
      "v_exp_f32 v105, v73\n"
      "v_mfma_f32_32x32x8_bf16 v[80:95], %[k2], %[q2b], v[80:95]\n"
      "ds_read_b128 %[k1], %[n16]\n"
      "ds_read_b64 %[k2], %[n8] offset:1024\n"
      "v_exp_f32 v106, v74\n"
      "v_exp_f32 v107, v75\n"
      "v_cvt_pk_bf16_f32 v100, v104, v105\n"
      "v_cvt_pk_bf16_f32 v101, v106, v107\n"
      "v_exp_f32 v108, v76\n"
      "v_exp_f32 v109, v77\n"
      "s_waitcnt lgkmcnt(3)\n"
      "v_mfma_f32_32x32x16_bf16 %[oa], %[v0], v[96:99], %[oa]\n"
      "v_exp_f32 v110, v78\n"
      "v_exp_f32 v111, v79\n"
      "v_cvt_pk_bf16_f32 v102, v108, v109\n"
      "v_cvt_pk_bf16_f32 v103, v110, v111\n"
      "v_exp_f32 v104, v80\n"
      "v_exp_f32 v105, v81\n"
      "s_waitcnt lgkmcnt(2)\n"
      "v_mfma_f32_32x32x16_bf16 %[oa], %[v1], v[100:103], %[oa]\n"
      "v_exp_f32 v106, v82\n"
      "v_exp_f32 v107, v83\n"
      "v_cvt_pk_bf16_f32 v96, v104, v105\n"
      "v_cvt_pk_bf16_f32 v97, v106, v107\n"
      "v_exp_f32 v108, v84\n"
      "v_exp_f32 v109, v85\n"
      "s_waitcnt lgkmcnt(1)\n"
      "v_mfma_f32_32x32x16_bf16 v[64:79], %[k1], %[q1a], 0\n"
      "v_exp_f32 v110, v86\n"
      "v_exp_f32 v111, v87\n"
      "v_cvt_pk_bf16_f32 v98, v108, v109\n"
      "v_cvt_pk_bf16_f32 v99, v110, v111\n"
      "v_exp_f32 v104, v88\n"
      "v_exp_f32 v105, v89\n"
      "s_waitcnt lgkmcnt(0)\n"
      "v_mfma_f32_32x32x8_bf16 v[64:79], %[k2], %[q2a], v[64:79]\n"
      "v_exp_f32 v106, v90\n"
      "v_exp_f32 v107, v91\n"
      "v_cvt_pk_bf16_f32 v100, v104, v105\n"
      "v_cvt_pk_bf16_f32 v101, v106, v107\n"
      "v_exp_f32 v108, v92\n"
      "v_exp_f32 v109, v93\n"
      "v_mfma_f32_32x32x16_bf16 %[ob], %[v0], v[96:99], %[ob]\n"
      "ds_read_b128 %[v0], %[n16] offset:1536\n"
      "v_exp_f32 v110, v94\n"
      "v_exp_f32 v111, v95\n"
      "v_cvt_pk_bf16_f32 v102, v108, v109\n"
      "v_cvt_pk_bf16_f32 v103, v110, v111\n"
      "s_mov_b32 %[tmp], %[b0]\n"
      "s_mov_b32 %[b0], %[b1]\n"
      "s_mov_b32 %[b1], %[b2]\n"
      "s_mov_b32 %[b2], %[tmp]\n"
      "s_add_u32 %[st], %[st], 1\n"
      "s_cmp_lt_u32 %[st], 8\n"
      "s_cbranch_scc1 1b\n"
      "s_nop 1\n"
      "v_mfma_f32_32x32x16_bf16 %[ob], %[v1], v[100:103], %[ob]\n"
      "s_waitcnt lgkmcnt(0)\n"
      "s_mov_b32 m0, %[keep]\n"
      "s_nop 15\n"
      : [oa] "+v"(oa), [ob] "+v"(ob), [k1] "=&v"(k1), [k2] "=&v"(k2), [v0] "=&v"(v0), [v1] "+v"(v1),
        [a16] "=&v"(a16), [a8] "=&v"(a8), [n16] "=&v"(n16), [n8] "=&v"(n8), [dv] "+v"(dv),
        [b0] "+s"(b0), [b1] "+s"(b1), [b2] "+s"(b2), [st] "=&s"(st), [tmp] "=&s"(tmp), [keep] "=&s"(keep),
        [rav] "+v"(rav), [rald] "+s"(rald)
      : [q1a] "v"(q1a), [q2a] "v"(q2a), [q1b] "v"(q1b), [q2b] "v"(q2b), [base16] "v"(base16), [base8] "v"(base8),
        [gp] "s"(KVf), [ldw] "s"(ldw), [rap] "s"(rap)
      : "memory", "scc", "v64", "v65", "v66", "v67", "v68", "v69", "v70", "v71", "v72", "v73", "v74", "v75", "v76", "v77", "v78", "v79", "v80", "v81", "v82", "v83", "v84", "v85", "v86", "v87", "v88", "v89", "v90", "v91", "v92", "v93", "v94", "v95", "v96", "v97", "v98", "v99", "v100", "v101", "v102", "v103", "v104", "v105", "v106", "v107", "v108", "v109", "v110", "v111");
  }
  if (w < 7) {
    const int grp = blockIdx.x >> 3;
    int ln = w * 64 + lane; ln = ln > 399 ? 399 : ln;
    dma4(grp >= 16 ? woB : woA, ((grp & 15) * 400 + ln) * 128, lbase + K2_SCR);
  }
  {
    const int l15 = lane & 15, g = lane >> 4, p4 = lane & 3, q4 = l15 >> 2;
    const int hh = w & 1, par = (w >> 1) & 1, yh = w >> 2;
    const char* R = lds + K2_RA + par * 256;
    auto ra = [&](int u, int B) { return R + u * 512 + ((((B >> 4) ^ u) & 15) << 4) + (B & 15); };
    bf16x8 kf[3]; s16x4 vt[3][2];
#pragma unroll
    for (int t = 0; t < 3; ++t) {
      int u = 16 * (2 * yh + t) + l15; u = u > 71 ? 71 : u;
      const int B = 80 + hh * 40 + (g < 3 ? 16 * g : 0);
      kf[t] = cat8(*(const s16x4*)ra(u, B), *(const s16x4*)ra(u, B + 8));
      int uv = 16 * (2 * yh + t) + 4 * g + q4; uv = uv > 71 ? 71 : uv;
      const int Bv = 160 + hh * 40 + p4 * 8;
      vt[t][0] = trread(ra(uv, Bv)); vt[t][1] = trread(ra(uv, Bv + 32 > 232 ? 232 : Bv + 32));
    }
    const f32x4 z4 = {0.f, 0.f, 0.f, 0.f};
#pragma unroll
    for (int yy = 0; yy < 2; ++yy) {
      const int y = 2 * yh + yy;
      const int uq = 8 + 16 * y + l15, Bq = hh * 40 + (g < 3 ? 16 * g : 0);
      s16x4 qlo = *(const s16x4*)ra(uq, Bq), qhi = *(const s16x4*)ra(uq, Bq + 8);
      const s16x4 zz = {0, 0, 0, 0};
      if (g == 3) qlo = zz;
      if (g >= 2) qhi = zz;
      const bf16x8 qf = cat8(qlo, qhi);
      const f32x4 s0v = mfma16(kf[yy], qf, z4), s1v = mfma16(kf[yy + 1], qf, z4);
      const bool first = (art == 0) && (y == 0);
      float mx = -3.0e38f;
      bool m0[4], m1[4];
#pragma unroll
      for (int r = 0; r < 4; ++r) {
        const int j = 4 * g + r;
        m0[r] = (j >= l15 + 1) && (j <= l15 + 8) && !(first && j < 8);
        m1[r] = (j <= l15 - 8);
        mx = fmaxf(mx, m0[r] ? s0v[r] : -3.0e38f);
        mx = fmaxf(mx, m1[r] ? s1v[r] : -3.0e38f);
      }
      mx = x16_max(mx);
      const float mc = mx * QSCALE;
      float p0[4], p1[4], ls = 0.f;
#pragma unroll
      for (int r = 0; r < 4; ++r) {
        p0[r] = m0[r] ? __builtin_amdgcn_exp2f(s0v[r] * QSCALE - mc) : 0.f;
        p1[r] = m1[r] ? __builtin_amdgcn_exp2f(s1v[r] * QSCALE - mc) : 0.f;
        ls += p0[r] + p1[r];
      }
      const float inv = 1.f / x16_sum(ls);
      bf16x8 pf;
#pragma unroll
      for (int r = 0; r < 4; ++r) { pf[r] = (__bf16)p0[r]; pf[4 + r] = (__bf16)p1[r]; }
      const f32x4 o0 = mfma16(cat8(vt[yy][0], vt[yy + 1][0]), pf, z4);
      const f32x4 o1 = mfma16(cat8(vt[yy][1], vt[yy + 1][1]), pf, z4);
      const int s = art * 128 - 16 + 2 * (8 + 16 * y + l15) + par;
      char* orow = ws + WS_OA + (size_t)s * 1280 + (2 * hp + hh) * 40;
      *(bf16x4*)(orow + g * 8) = pack4(o0[0] * inv, o0[1] * inv, o0[2] * inv, o0[3] * inv);
      if (g == 0) *(bf16x4*)(orow + 32) = pack4(o1[0] * inv, o1[1] * inv, o1[2] * inv, o1[3] * inv);
    }
  }
  BAR();
  {
    float* ex = (float*)(lds + (pr * 2 + (kh ^ 1)) * 3072);
    const f32x16& give = kh ? oa : ob;
#pragma unroll
    for (int r4 = 0; r4 < 3; ++r4) {
      f32x4 v = {give[4 * r4], give[4 * r4 + 1], give[4 * r4 + 2], give[4 * r4 + 3]};
      *(f32x4*)(ex + r4 * 256 + lane * 4) = v;
    }
  }
  BAR();
  {
    const int h = lane & 31, hf = lane >> 5;
    const float* ex = (const float*)(lds + (pr * 2 + kh) * 3072);
    const f32x16& mine = kh ? ob : oa;
    float o[12];
#pragma unroll
    for (int r4 = 0; r4 < 3; ++r4) {
      f32x4 v = *(const f32x4*)(ex + r4 * 256 + lane * 4);
#pragma unroll
      for (int r = 0; r < 4; ++r) o[4 * r4 + r] = mine[4 * r4 + r] + v[r];
    }
    const int s = s0 + kh;
    const float l = x32_sum(hf ? o[8] : 0.f);
    char* orow = ws + WS_OB + (size_t)s * 1280 + h * 40;
    const bool bad = !(l >= 8.6736174e-19f) || !(l <= 3.0e38f);
    if (__any(bad)) {
      const char* qb = ws + WS_QP + (size_t)(s >> 5) * 40 * 1024 + (s & 31) * 32;
      float q[20], oo[20];
#pragma unroll
      for (int d = 0; d < 20; ++d) { const int col = 20 * h + d; q[d] = bf2f(*(const __bf16*)(qb + (col >> 4) * 1024 + (col & 15) * 2)); oo[d] = 0.f; }
      float m = -3.0e38f, ls = 0.f;
#pragma unroll 1
      for (int kk = 0; kk < S_; ++kk) {
        const char* tb = KVf + (size_t)(kk >> 5) * KV_TILE;
        const int kl = kk & 31;
        float sc = 0.f;
#pragma unroll
        for (int d = 0; d < 16; ++d) sc += q[d] * bf2f(*(const __bf16*)(tb + (kl + 32 * (d >> 3)) * 16 + (d & 7) * 2));
#pragma unroll
        for (int d = 16; d < 20; ++d) sc += q[d] * bf2f(*(const __bf16*)(tb + 1024 + kl * 8 + (d - 16) * 2));
        const float mn = fmaxf(m, sc), al = __builtin_amdgcn_exp2f(m - mn), p = __builtin_amdgcn_exp2f(sc - mn);
        const int sx = kl >> 4, k16 = kl & 15, vh = (k16 >> 2) & 1, j = (k16 & 3) + 4 * (k16 >> 3);
        ls = ls * al + p;
#pragma unroll
        for (int d = 0; d < 20; ++d) oo[d] = oo[d] * al + p * bf2f(*(const __bf16*)(tb + 1536 + sx * 1024 + (d + 32 * vh) * 16 + j * 2));
        m = mn;
      }
      const float inv = 1.f / ls;
      if (hf == 0) {
#pragma unroll
        for (int d4 = 0; d4 < 5; ++d4)
          *(bf16x4*)(orow + d4 * 8) = pack4(oo[4 * d4] * inv, oo[4 * d4 + 1] * inv, oo[4 * d4 + 2] * inv, oo[4 * d4 + 3] * inv);
      }
    } else {
      const float inv = 1.f / l;
      *(bf16x4*)(orow + hf * 8) = pack4(o[0] * inv, o[1] * inv, o[2] * inv, o[3] * inv);
      *(bf16x4*)(orow + 16 + hf * 8) = pack4(o[4] * inv, o[5] * inv, o[6] * inv, o[7] * inv);
      if (hf == 0) *(bf16x4*)(orow + 32) = pack4(o[8] * inv, o[9] * inv, o[10] * inv, o[11] * inv);
    }
  }
}

constexpr int K3_XB = 24576;

__global__ __launch_bounds__(512, 2) void k3_out(const __bf16* __restrict__ woA, const __bf16* __restrict__ woB,
                                                 char* __restrict__ ws, __bf16* __restrict__ out) {
  __shared__ __attribute__((aligned(16))) char lds[G_NB * G_STAGE];
  const int tid = threadIdx.x, lane = tid & 63, w = __builtin_amdgcn_readfirstlane(tid >> 6);
  const int T = (blockIdx.x & 7) * 28 + (blockIdx.x >> 3);
  if (T >= 220) return;
  const int rt = T / 10, ct = T - rt * 10;
  const unsigned lbase = (unsigned)(size_t)lds;
  const int l15 = lane & 15, g = lane >> 4;
  if (w >= 4) {
    const int lw = w - 4;
    const int rX = ct;
    unsigned aoff[6], woff[4], al[6];
#pragma unroll
    for (int ii = 0; ii < 6; ++ii) {
      int p = lw * 6 + ii + rX; p = p >= 24 ? p - 24 : p;
      const int row = 4 * p + (lane >> 4);
      int s = rt * 96 + row; s = s > S_ - 1 ? S_ - 1 : s;
      aoff[ii] = s * 1280 + (((lane & 15) ^ (row & 15)) << 4);
      al[ii] = p * 1024;
    }
#pragma unroll
    for (int ii = 0; ii < 4; ++ii) {
      const int k = 8 * (lw * 4 + ii) + (lane >> 3), cp = lane & 7;
      woff[ii] = k * 1280 + ct * 128 + (((cp >> 1) ^ (((k >> 1) & 1) | (((k >> 3) & 1) << 1))) << 5) + ((cp & 1) << 4);
    }
    auto issue = [&](int st, int buf) {
      const int part = st >= 5, kk = st - 5 * part;
      const char* ab = ws + (part ? WS_OB : WS_OA) + kk * 256;
      const char* wp = (const char*)(part ? woB : woA) + (size_t)kk * 128 * 1280;
      const unsigned lb = lbase + buf * G_STAGE;
      const unsigned wl = lb + K3_XB + lw * 4096;
      dma16(ab, aoff[0], lb + al[0]); dma16(wp, woff[0], wl); dma16(ab, aoff[1], lb + al[1]);
      dma16(ab, aoff[2], lb + al[2]); dma16(wp, woff[1], wl + 1024); dma16(ab, aoff[3], lb + al[3]);
      dma16(wp, woff[2], wl + 2048); dma16(ab, aoff[4], lb + al[4]); dma16(ab, aoff[5], lb + al[5]);
      dma16(wp, woff[3], wl + 3072);
    };
    issue(0, 0); issue(1, 1); issue(2, 2);
    WAIT_BAR(10);
#pragma unroll 1
    for (int st = 0; st < 9; ++st) {
      if (st < 7) { issue(st + 3, (st + 3) & 3); WAIT_BAR(10); }
      else { WAIT_BAR(0); }
    }
  } else {
    const int wm = w >> 1, wn = w & 1;
    const int p4 = lane & 3, q4 = l15 >> 2;
    const int xo0 = (wm * 48 + l15) * 256 + ((g ^ l15) << 4);
    const int f = ((q4 >> 1) & 1) | ((g & 1) << 1);
    int wb[2];
#pragma unroll
    for (int j = 0; j < 2; ++j) wb[j] = K3_XB + (8 * g + q4) * 128 + 8 * p4 + 32 * ((2 * wn + j) ^ f);
    float gv[3];
#pragma unroll
    for (int mi = 0; mi < 3; ++mi) {
      int s = rt * 96 + wm * 48 + mi * 16 + l15; s = s > S_ - 1 ? S_ - 1 : s;
      const float gg = ((const float*)(ws + WS_G))[s];
      gv[mi] = gg;
    }
    f32x4 acc[3][2] = {}, accA[3][2];
    bf16x8 xf[4][3], wf[4][2];
    auto ldf = [&](int set, const char* sb, int ks) {
#pragma unroll
      for (int mi = 0; mi < 3; ++mi) xf[set][mi] = *(const bf16x8*)(sb + (xo0 ^ (ks * 64)) + mi * 4096);
#pragma unroll
      for (int ni = 0; ni < 2; ++ni) {
        const char* pw = sb + wb[ni] + ks * 32 * 128;
        wf[set][ni] = cat8(trread(pw), trread(pw + 4 * 128));
      }
    };
    auto mm = [&](int set) {
#pragma unroll
      for (int mi = 0; mi < 3; ++mi)
#pragma unroll
        for (int ni = 0; ni < 2; ++ni) acc[mi][ni] = mfma16(wf[set][ni], xf[set][mi], acc[mi][ni]);
#pragma unroll
      for (int i = 0; i < 6; ++i) { __builtin_amdgcn_sched_group_barrier(0x8, 1, 0); __builtin_amdgcn_sched_group_barrier(0x100, 1, 0); }
      __builtin_amdgcn_sched_group_barrier(0x100, 1, 0);
      __builtin_amdgcn_sched_barrier(0);
    };
    WAIT_BAR(10);
    ldf(0, lds, 0); ldf(1, lds, 1);
#pragma unroll
    for (int st = 0; st < 10; ++st) {
      const char* sb = lds + (st & 3) * G_STAGE;
      const char* sn = lds + ((st + 1) & 3) * G_STAGE;
      if (st == 5) {
#pragma unroll
        for (int mi = 0; mi < 3; ++mi)
#pragma unroll
          for (int ni = 0; ni < 2; ++ni) { accA[mi][ni] = acc[mi][ni] * gv[mi]; acc[mi][ni] = f32x4{0.f, 0.f, 0.f, 0.f}; }
      }
      ldf(2, sb, 2); mm(0);
      ldf(3, sb, 3); mm(1);
      if (st < 9) ldf(0, sn, 0);
      mm(2);
      if (st < 9) ldf(1, sn, 1);
      mm(3);
      if (st < 9) RAWBAR();
    }
    const int s0 = rt * 96 + wm * 48 + l15;
    char* dst = (char*)out + (size_t)s0 * 1280 + ct * 128 + (wn * 32 + (g & 1) * 16 + (g >> 1) * 8) * 2;
#pragma unroll
    for (int mi = 0; mi < 3; ++mi) {
      const u32x4 v = pair_pack(accA[mi][0] + acc[mi][0] * (1.f - gv[mi]), accA[mi][1] + acc[mi][1] * (1.f - gv[mi]));
      if (s0 + mi * 16 < S_) *(u32x4*)(dst + (size_t)mi * 16 * 1280) = v;
    }
  }
}

extern "C" void kernel_launch(void* const* d_in, const int* in_sizes, int n_in, void* d_out, int out_size, void* d_ws,
                              size_t ws_size, hipStream_t stream) {
  const __bf16* x = (const __bf16*)d_in[0];
  const __bf16* wqkv = (const __bf16*)d_in[1];
  const __bf16* woA = (const __bf16*)d_in[2];
  const __bf16* wq = (const __bf16*)d_in[4];
  const __bf16* wk = (const __bf16*)d_in[5];
  const __bf16* wv = (const __bf16*)d_in[6];
  const __bf16* woB = (const __bf16*)d_in[7];
  const __bf16* gw = (const __bf16*)d_in[8];
  char* ws = (char*)d_ws;
  k1_proj<<<240, 512, 0, stream>>>(x, wqkv, wq, wk, wv, gw, ws);
  k2_attn<<<256, 512, 0, stream>>>(ws, (const char*)woA, (const char*)woB);
  k3_out<<<224, 512, 0, stream>>>(woA, woB, ws, (__bf16*)d_out);
}
```

```cpp
#include <hip/hip_runtime.h>
#include <hip/hip_bf16.h>
#include <stdint.h>

#define DI __device__ __forceinline__
typedef __attribute__((ext_vector_type(8))) __bf16 bf16x8;
typedef __attribute__((ext_vector_type(4))) __bf16 bf16x4;
typedef __attribute__((ext_vector_type(4))) short s16x4;
typedef __attribute__((ext_vector_type(8))) short s16x8;
typedef __attribute__((ext_vector_type(16))) float f32x16;
typedef __attribute__((ext_vector_type(4))) float f32x4;
typedef __attribute__((ext_vector_type(4))) unsigned u32x4;
typedef __attribute__((ext_vector_type(2))) unsigned u32x2;
typedef __attribute__((address_space(3))) s16x4* lds_s16x4_ptr;

constexpr int S_ = 2048, D_ = 640;
constexpr float QSCALE = 0.22360679774997896f * 1.4426950408889634f;

constexpr size_t WS_QP = 0;
constexpr size_t WS_QKV = 2048 * 1280;
constexpr size_t WS_KV = WS_QKV + 2048 * 3840;
constexpr size_t WS_G = 13u << 20;
constexpr size_t WS_KMAX = (13u << 20) + 65536;
constexpr size_t WS_OA = 32u << 20;
constexpr size_t WS_OB = 36u << 20;
constexpr int KV_TILE = 3584;

DI f32x4 mfma16(bf16x8 a, bf16x8 b, f32x4 c) { return __builtin_amdgcn_mfma_f32_16x16x32_bf16(a, b, c, 0, 0, 0); }
DI f32x16 mfma32(bf16x8 a, bf16x8 b, f32x16 c) { return __builtin_amdgcn_mfma_f32_32x32x16_bf16(a, b, c, 0, 0, 0); }
DI s16x4 trread(const char* p) { return __builtin_amdgcn_ds_read_tr16_b64_v4i16((lds_s16x4_ptr)p); }
DI bf16x8 cat8(s16x4 lo, s16x4 hi) { return __builtin_bit_cast(bf16x8, __builtin_shufflevector(lo, hi, 0, 1, 2, 3, 4, 5, 6, 7)); }
DI bf16x4 pack4(float a, float b, float c, float d) { bf16x4 r; r[0] = (__bf16)a; r[1] = (__bf16)b; r[2] = (__bf16)c; r[3] = (__bf16)d; return r; }
DI float bf2f(__bf16 v) { return (float)v; }
DI float x16_max(float x) {
  auto s = __builtin_amdgcn_permlane16_swap(__float_as_uint(x), __float_as_uint(x), false, false);
  x = fmaxf(__uint_as_float(s[0]), __uint_as_float(s[1]));
  auto t = __builtin_amdgcn_permlane32_swap(__float_as_uint(x), __float_as_uint(x), false, false);
  return fmaxf(__uint_as_float(t[0]), __uint_as_float(t[1]));
}
DI float x16_sum(float x) {
  auto s = __builtin_amdgcn_permlane16_swap(__float_as_uint(x), __float_as_uint(x), false, false);
  x = __uint_as_float(s[0]) + __uint_as_float(s[1]);
  auto t = __builtin_amdgcn_permlane32_swap(__float_as_uint(x), __float_as_uint(x), false, false);
  return __uint_as_float(t[0]) + __uint_as_float(t[1]);
}
DI float x32_sum(float x) {
  auto t = __builtin_amdgcn_permlane32_swap(__float_as_uint(x), __float_as_uint(x), false, false);
  return __uint_as_float(t[0]) + __uint_as_float(t[1]);
}
template <int CTRL> DI float dppf(float x) { return __builtin_bit_cast(float, __builtin_amdgcn_mov_dpp(__builtin_bit_cast(int, x), CTRL, 0xf, 0xf, true)); }
DI float row16_max(float x) {
  x = fmaxf(x, dppf<0xB1>(x)); x = fmaxf(x, dppf<0x4E>(x)); x = fmaxf(x, dppf<0x141>(x)); x = fmaxf(x, dppf<0x140>(x));
  return x;
}

DI u32x4 pair_pack(const f32x4& a, const f32x4& b) {
  u32x2 pa = __builtin_bit_cast(u32x2, pack4(a[0], a[1], a[2], a[3])), pb = __builtin_bit_cast(u32x2, pack4(b[0], b[1], b[2], b[3]));
  auto s0 = __builtin_amdgcn_permlane16_swap(pa[0], pb[0], false, false);
  auto s1 = __builtin_amdgcn_permlane16_swap(pa[1], pb[1], false, false);
  return u32x4{s0[0], s1[0], s0[1], s1[1]};
}
DI u32x4 quad_pack(const f32x16& a, int q) {
  u32x2 pa = __builtin_bit_cast(u32x2, pack4(a[4 * q], a[4 * q + 1], a[4 * q + 2], a[4 * q + 3]));
  u32x2 pb = __builtin_bit_cast(u32x2, pack4(a[4 * q + 4], a[4 * q + 5], a[4 * q + 6], a[4 * q + 7]));
  auto s0 = __builtin_amdgcn_permlane32_swap(pa[0], pb[0], false, false);
  auto s1 = __builtin_amdgcn_permlane32_swap(pa[1], pb[1], false, false);
  return u32x4{s0[0], s1[0], s0[1], s1[1]};
}
DI const void* uni_ptr(const void* p) {
  const unsigned long long a = (unsigned long long)p;
  const unsigned lo = __builtin_amdgcn_readfirstlane((unsigned)a), hi = __builtin_amdgcn_readfirstlane((unsigned)(a >> 32));
  return (const void*)(((unsigned long long)hi << 32) | lo);
}
DI void dma16(const void* sbase, unsigned voff, unsigned lds_dst) {
  sbase = uni_ptr(sbase); lds_dst = __builtin_amdgcn_readfirstlane(lds_dst);
  unsigned keep;
  asm volatile("s_mov_b32 %0, m0\n\ts_mov_b32 m0, %3\n\ts_nop 2\n\tglobal_load_lds_dwordx4 %1, %2\n\ts_mov_b32 m0, %0"
               : "=&s"(keep) : "v"(voff), "s"(sbase), "s"(lds_dst) : "memory");
}
DI void dma4(const void* sbase, unsigned voff, unsigned lds_dst) {
  sbase = uni_ptr(sbase); lds_dst = __builtin_amdgcn_readfirstlane(lds_dst);
  unsigned keep;
  asm volatile("s_mov_b32 %0, m0\n\ts_mov_b32 m0, %3\n\ts_nop 2\n\tglobal_load_lds_dword %1, %2\n\ts_mov_b32 m0, %0"
               : "=&s"(keep) : "v"(voff), "s"(sbase), "s"(lds_dst) : "memory");
}
#define WAIT_BAR(n) asm volatile("s_waitcnt vmcnt(" #n ") lgkmcnt(0)\n\ts_barrier" ::: "memory")
#define BAR() asm volatile("s_waitcnt lgkmcnt(0)\n\ts_barrier" ::: "memory")
#define RAWBAR() asm volatile("s_barrier" ::: "memory")

constexpr int G_STAGE = 40960, G_NB = 4;
constexpr int K1_XB = 16384;

DI void k1_kv_block(char* lds, int kvb, int w, int lane, const __bf16* x, const __bf16* wk, const __bf16* wv,
                    const __bf16* gw, char* KVf, float* gout, float* kmax2) {
  const unsigned lbase = (unsigned)(size_t)lds;
  const int tid = w * 64 + lane, l15 = lane & 15, g = lane >> 4;
  constexpr int WK = 81920, WV = WK + 25600, GW = WV + 25600;
  {
    const char* xb = (const char*)x + (size_t)kvb * 64 * 1280;
#pragma unroll 2
    for (int p = w; p < 80; p += 8) {
      const int q = p * 64 + lane, row = q / 80, cp = q - row * 80;
      dma16(xb, row * 1280 + (((cp & ~7) | ((cp ^ row) & 7)) << 4), lbase + p * 1024);
    }
    for (int p = w; p < 50; p += 8) {
      const char* src = (p < 25) ? (const char*)wk + p * 1024 : (const char*)wv + (p - 25) * 1024;
      dma16(src, lane * 16, lbase + WK + p * 1024);
    }
    for (int k = tid; k < 640; k += 512) {
      bf16x4 v; v[0] = gw[k]; v[1] = (__bf16)0.f; v[2] = (__bf16)0.f; v[3] = (__bf16)0.f;
      *(bf16x4*)(lds + GW + k * 40) = v;
    }
  }
  WAIT_BAR(0);
  f32x4 acc[3] = {};
  const int row = (w & 3) * 16 + l15;
  if (w < 4) {
    const int p4 = lane & 3, q4 = l15 >> 2;
    int wa[3];
#pragma unroll
    for (int ni = 0; ni < 3; ++ni) {
      const int cg = 4 * ni + p4;
      wa[ni] = (cg < 5 ? WK + cg * 8 : (cg < 10 ? WV + (cg - 5) * 8 : GW)) + (8 * g + q4) * 40;
    }
    const int xo = row * 1280 + ((g ^ (row & 7)) << 4);
#pragma unroll
    for (int ks = 0; ks < 20; ++ks) {
      const bf16x8 xf = *(const bf16x8*)(lds + (xo ^ ((ks & 1) * 64)) + (ks >> 1) * 128);
#pragma unroll
      for (int ni = 0; ni < 3; ++ni) {
        const char* pw = lds + wa[ni] + ks * 32 * 40;
        acc[ni] = mfma16(cat8(trread(pw), trread(pw + 160)), xf, acc[ni]);
      }
    }
  }
  BAR();
  if (w < 4) {
#pragma unroll
    for (int ni = 0; ni < 3; ++ni) {
      const int cg = 4 * ni + g;
      const bf16x4 v = pack4(acc[ni][0], acc[ni][1], acc[ni][2], acc[ni][3]);
      if (cg < 5) *(bf16x4*)(lds + row * 96 + cg * 8) = v;
      else if (cg < 10) *(bf16x4*)(lds + row * 96 + 48 + (cg - 5) * 8) = v;
      else if (cg == 10) gout[kvb * 64 + row] = 1.f / (1.f + __expf(-acc[ni][0]));
    }
  }
  BAR();
  if (w >= 2) return;
  const char* T = lds + w * 32 * 96;
  const int t = kvb * 2 + w;
  char* dst = KVf + (size_t)t * KV_TILE;
  const int key = lane & 31, hf = lane >> 5;
  const u32x4 k1 = *(const u32x4*)(T + key * 96 + hf * 16);
  u32x2 k2 = *(const u32x2*)(T + key * 96 + 32);
  {
    const bf16x8 a = __builtin_bit_cast(bf16x8, k1); const bf16x4 b = __builtin_bit_cast(bf16x4, k2);
    float n = 0.f;
#pragma unroll
    for (int j = 0; j < 8; ++j) n += bf2f(a[j]) * bf2f(a[j]);
    float n3 = 0.f;
#pragma unroll
    for (int j = 0; j < 4; ++j) n3 += bf2f(b[j]) * bf2f(b[j]);
    n = x32_sum(n) + n3;
    n = x16_max(row16_max(n));
    if (lane == 0) kmax2[t] = n;
  }
  *(u32x4*)(dst + lane * 16) = k1;
  if (hf) { k2[0] = 0x3F80u; k2[1] = 0u; }
  *(u32x2*)(dst + 1024 + lane * 8) = k2;
#pragma unroll
  for (int s = 0; s < 2; ++s) {
    s16x8 v;
#pragma unroll
    for (int j = 0; j < 8; ++j) {
      const int kk = 16 * s + 8 * (j >> 2) + 4 * hf + (j & 3);
      const short e = *(const short*)(T + kk * 96 + 48 + (key < 20 ? key : 0) * 2);
      v[j] = key < 20 ? e : (key == 20 ? (short)0x3F80 : (short)0);
    }
    *(s16x8*)(dst + 1536 + s * 1024 + lane * 16) = v;
  }
}

__global__ __launch_bounds__(512, 2) void k1_proj(const __bf16* __restrict__ x, const __bf16* __restrict__ wqkv,
                                                  const __bf16* __restrict__ wq, const __bf16* __restrict__ wk,
                                                  const __bf16* __restrict__ wv, const __bf16* __restrict__ gw,
                                                  char* __restrict__ ws) {
  __shared__ __attribute__((aligned(16))) char lds[G_NB * G_STAGE];
  const int tid = threadIdx.x, lane = tid & 63, w = __builtin_amdgcn_readfirstlane(tid >> 6);
  const int b = blockIdx.x, xcd = b & 7, idx = b >> 3;
  if (idx >= 28) {
    k1_kv_block(lds, xcd * 4 + (idx - 28), w, lane, x, wk, wv, gw, ws + WS_KV, (float*)(ws + WS_G), (float*)(ws + WS_KMAX));
    return;
  }
  const int rt = 4 * (xcd >> 1) + idx / 7, ct = 7 * (xcd & 1) + idx % 7;
  const unsigned lbase = (unsigned)(size_t)lds;
  const int l15 = lane & 15, g = lane >> 4;
  if (w >= 4) {
    const int lw = w - 4;
    const bool isq = ct >= 10;
    const char* wb = isq ? (const char*)wq : (const char*)wqkv;
    const unsigned wstride = isq ? 1280u : 3840u;
    const int col0 = isq ? (ct - 10) * 192 : ct * 192;
    const int rX = idx % 7, rW = (0x5320 >> (4 * (idx / 7))) & 15;
    unsigned xoff[4], woff[6], xl[4], wl[6];
#pragma unroll
    for (int ii = 0; ii < 4; ++ii) {
      const int p = (lw * 4 + ii + rX) & 15;
      const int row = 8 * p + (lane >> 3);
      xoff[ii] = (rt * 128 + row) * 1280 + (((lane & 7) ^ ((row >> 1) & 7)) << 4);
      xl[ii] = p * 1024;
    }
#pragma unroll
    for (int ii = 0; ii < 6; ++ii) {
      int p = lw * 6 + ii + rW; p = p >= 24 ? p - 24 : p;
      const int q = 64 * p + lane, k = q / 24, cp = q - k * 24;
      int col = col0 + (((cp >> 1) ^ (k & 2)) << 4) + ((cp & 1) << 3);
      if (isq && col > 632) col = 632;
      woff[ii] = k * wstride + col * 2;
      wl[ii] = K1_XB + p * 1024;
    }
    auto issue = [&](int st, int buf) {
      const char* xb = (const char*)x + st * 128;
      const char* wp = wb + (size_t)st * 64 * wstride;
      const unsigned lb = lbase + buf * G_STAGE;
      dma16(xb, xoff[0], lb + xl[0]); dma16(wp, woff[0], lb + wl[0]); dma16(wp, woff[1], lb + wl[1]);
      dma16(xb, xoff[1], lb + xl[1]); dma16(wp, woff[2], lb + wl[2]); dma16(xb, xoff[2], lb + xl[2]);
      dma16(wp, woff[3], lb + wl[3]); dma16(wp, woff[4], lb + wl[4]); dma16(xb, xoff[3], lb + xl[3]);
      dma16(wp, woff[5], lb + wl[5]);
    };
    issue(0, 0); issue(1, 1); issue(2, 2);
    WAIT_BAR(10);
#pragma unroll 1
    for (int st = 0; st < 9; ++st) {
      if (st < 7) { issue(st + 3, (st + 3) & 3); WAIT_BAR(10); }
      else { WAIT_BAR(0); }
    }
  } else {
    const int wm = w >> 1, wn = w & 1;
    const int l31 = lane & 31, h = lane >> 5, p4 = lane & 3, q4 = l15 >> 2;
    const int xo0 = (wm * 64 + l31) * 128 + ((h ^ ((l31 >> 1) & 7)) << 4);
    const int wo0 = K1_XB + (8 * h + q4) * 384 + 8 * p4 + 32 * ((6 * wn + (g & 1)) ^ (q4 & 2));
    const int wo1 = K1_XB + (8 * h + q4) * 384 + 8 * p4 + 32 * ((6 * wn + 2 + (g & 1)) ^ (q4 & 2));
    f32x16 acc[2][3] = {};
    bf16x8 xa[2], wa[3], xb[2], wc[3];
    auto ldf = [&](bf16x8 (&xf)[2], bf16x8 (&wf)[3], const char* sb, int ks) {
#pragma unroll
      for (int mi = 0; mi < 2; ++mi) xf[mi] = *(const bf16x8*)(sb + (xo0 ^ (ks * 32)) + mi * 4096);
#pragma unroll
      for (int ni = 0; ni < 3; ++ni) {
        const char* pw = sb + (ni == 1 ? wo1 : wo0) + (ni == 2 ? 128 : 0) + ks * 16 * 384;
        wf[ni] = cat8(trread(pw), trread(pw + 4 * 384));
      }
    };
    auto mm = [&](const bf16x8 (&xf)[2], const bf16x8 (&wf)[3]) {
#pragma unroll
      for (int mi = 0; mi < 2; ++mi)
#pragma unroll
        for (int ni = 0; ni < 3; ++ni) acc[mi][ni] = mfma32(wf[ni], xf[mi], acc[mi][ni]);
#pragma unroll
      for (int i = 0; i < 4; ++i) { __builtin_amdgcn_sched_group_barrier(0x8, 1, 0); __builtin_amdgcn_sched_group_barrier(0x100, 2, 0); }
      __builtin_amdgcn_sched_group_barrier(0x8, 2, 0);
      __builtin_amdgcn_sched_barrier(0);
    };
    WAIT_BAR(10);
    ldf(xa, wa, lds, 0);
#pragma unroll
    for (int st = 0; st < 10; ++st) {
      const char* sb = lds + (st & 3) * G_STAGE;
      const char* sn = lds + ((st + 1) & 3) * G_STAGE;
      ldf(xb, wc, sb, 1); mm(xa, wa);
      ldf(xa, wa, sb, 2); mm(xb, wc);
      ldf(xb, wc, sb, 3); mm(xa, wa);
      if (st < 9) ldf(xa, wa, sn, 0);
      mm(xb, wc);
      if (st < 9) RAWBAR();
    }
    char* dst; int no, oc0;
    if (ct >= 10) {
      dst = ws + WS_QP; no = 80; oc0 = (ct - 10) * 24;
#pragma unroll
      for (int mi = 0; mi < 2; ++mi)
#pragma unroll
        for (int ni = 0; ni < 3; ++ni) acc[mi][ni] *= QSCALE;
    } else { dst = ws + WS_QKV; no = 240; oc0 = ct * 24; }
    oc0 += wn * 12;
    dst += (size_t)((rt * 4 + wm * 2) * no) * 512 + lane * 16;
#pragma unroll
    for (int mi = 0; mi < 2; ++mi)
#pragma unroll
      for (int ni = 0; ni < 3; ++ni)
#pragma unroll
        for (int qq = 0; qq < 2; ++qq) {
          const u32x4 v = quad_pack(acc[mi][ni], 2 * qq);
          const int oc = oc0 + 4 * ni + 2 * qq;
          if (oc < no) *(u32x4*)(dst + (size_t)(mi * no + oc) * 512) = v;
        }
  }
}

constexpr int K2_STAGE = 8 * KV_TILE;
constexpr unsigned A_START = 0;
constexpr int K2_RA = 3 * K2_STAGE;
constexpr int K2_QI = K2_RA + 38 * 1024;
constexpr int K2_EX = K2_QI + 10240;
constexpr int K2_FL = K2_EX + 8 * 3072;
constexpr int K2_SCR = K2_FL + 2304;

DI void prep_q(const char* qi, int lane, float km, bf16x8& q1, s16x4& q2) {
  const int h = lane & 31, hf = lane >> 5;
  auto qa = [&](int col) { return *(const u32x2*)(qi + (col >> 3) * 128 + (col & 7) * 2); };
  const u32x2 lo = qa(20 * h + 8 * hf), hi = qa(20 * h + 8 * hf + 4), tl = qa(20 * h + 16);
  u32x4 qq = {lo[0], lo[1], hi[0], hi[1]};
  q1 = __builtin_bit_cast(bf16x8, qq);
  bf16x4 t4 = __builtin_bit_cast(bf16x4, tl);
  float n2 = 0.f;
#pragma unroll
  for (int j = 0; j < 8; ++j) { float v = bf2f(q1[j]); n2 += v * v; }
  float n3 = 0.f;
#pragma unroll
  for (int j = 0; j < 4; ++j) { float v = bf2f(t4[j]); n3 += v * v; }
  n2 = x32_sum(n2) + n3;
  const float off = __builtin_sqrtf(n2 * km) * 1.01f - 60.f;
  unsigned ob = __float_as_uint(off);
  ob = (off > 0.f) ? ((ob + 0xFFFFu) & 0xFFFF0000u) : (ob & 0xFFFF0000u);
  u32x2 t2 = tl;
  if (hf) { t2[0] = (ob >> 16) ^ 0x8000u; t2[1] = 0u; }
  q2 = __builtin_bit_cast(s16x4, t2);
}

__global__ __launch_bounds__(832, 4) void k2_attn(char* __restrict__ ws, const char* __restrict__ woA, const char* __restrict__ woB) {
  __shared__ __attribute__((aligned(16))) char lds[K2_SCR + 256];
  const int tid = threadIdx.x, lane = tid & 63, w = __builtin_amdgcn_readfirstlane(tid >> 6);
  const unsigned lbase = (unsigned)(size_t)lds;
  const char* KVf = ws + WS_KV;
  volatile unsigned* fl = (volatile unsigned*)(lds + K2_FL);
  const int pr = w & 3, kh = (w >> 2) & 1;
  const int sb = blockIdx.x * 8;
  const int hp = blockIdx.x & 15, art = blockIdx.x >> 4;
  if (w < 8) {
    const char* qp = ws + WS_QP + (size_t)(sb >> 5) * 80 * 512 + (sb & 31) * 16;
#pragma unroll
    for (int ii = 0; ii < 2; ++ii) {
      int p = w + 8 * ii; p = p > 9 ? 9 : p;
      dma16(qp, (8 * p + (lane >> 3)) * 512 + (lane & 7) * 16, lbase + K2_QI + p * 1024);
    }
    const int wq = w > 6 ? 6 : w;
#pragma unroll
    for (int k = 0; k < 4; ++k) dma16(KVf + wq * 4096 + k * 1024, lane * 16, lbase + wq * 4096 + k * 1024);
  } else {
    for (int i = tid - 512; i < 576; i += 320) fl[i] = i == 0 ? 1u : 0u;
  }
  float km = 0.f;
  if (w < 8) km = ((const float*)(ws + WS_KMAX))[lane];
  WAIT_BAR(0);
  if (w == 8) {
    __builtin_amdgcn_s_setprio(3);
    const unsigned dv = lane * 16;
#pragma unroll 1
    for (int st = 1; st < 8; ++st) {
      if (st >= 3) {
        while (true) {
          const unsigned d = fl[64 + 64 * (lane & 7)];
          if (__all(d >= (unsigned)(st - 2))) break;
          __builtin_amdgcn_s_sleep(2);
        }
      }
      const char* src = KVf + st * K2_STAGE;
      const unsigned ld = lbase + (st % 3) * K2_STAGE;
#pragma unroll 4
      for (int k = 0; k < 28; ++k) dma16(src + k * 1024, dv, ld + k * 1024);
      asm volatile("s_waitcnt vmcnt(0)" ::: "memory");
      fl[0] = st + 1;
      if (st == 4) {
        const int grp = blockIdx.x >> 3;
#pragma unroll
        for (int k = 0; k < 7; ++k) {
          int ln = k * 64 + lane; ln = ln > 399 ? 399 : ln;
          dma4(grp >= 16 ? woB : woA, ((grp & 15) * 400 + ln) * 128, lbase + K2_SCR);
        }
      }
    }
    return;
  }
  if (w > 8) {
    {
      const char* rap = ws + WS_QKV + ((long)art * 4 - 1) * 240 * 512 + hp * 5 * 512;
      unsigned rp[3];
#pragma unroll
      for (int t = 0; t < 3; ++t) { const int r = 16 + 64 * t + lane; rp[t] = (r >> 5) * 240 * 512 + (r & 31) * 16; }
#pragma unroll 1
      for (int co = w - 9; co < 15; co += 4) {
        const int seg = (co * 13) >> 6;
        const char* src = rap + (seg * 75 + co) * 512;
        const unsigned ld = lbase + K2_RA + co * 2560;
        dma16(src, rp[0], ld); dma16(src, rp[1], ld + 1024);
        if (lane < 32) dma16(src, rp[2], ld + 2048);
      }
      asm volatile("s_waitcnt vmcnt(0)" ::: "memory");
      if (lane == 0) __hip_atomic_fetch_add((unsigned*)(lds + K2_FL + 4), 1u, __ATOMIC_RELAXED, __HIP_MEMORY_SCOPE_WORKGROUP);
    }
    while (fl[64 + 64 * (w & 3)] < A_START) __builtin_amdgcn_s_sleep(16);
    while (fl[1] < 4u) __builtin_amdgcn_s_sleep(8);
    const int l15 = lane & 15, g = lane >> 4, p4 = lane & 3, q4 = l15 >> 2;
    const int hh = w & 1, par = ((w - 9) >> 1) & 1;
    constexpr int TS = 4 * 128;
    const char* RA = lds + K2_RA;
    auto ea = [&](int u, int cc) { const int i = 2 * u + par; return RA + ((cc >> 3) * 20 + (i >> 3)) * 128 + (i & 7) * 16 + (cc & 7) * 2; };
    const int gk = g < 3 ? 8 * g : 0;
    const char* klo = ea(l15, 40 + 20 * hh + gk);
    const char* khi = ea(l15, 40 + 20 * hh + gk + 4);
    const char* vp0 = ea(4 * g + q4, 80 + 20 * hh + 4 * p4);
    const char* vp1 = ea(4 * g + q4, 80 + 20 * hh + 16 + (hh == 1 && p4 > 0 ? 0 : 4 * p4));
    const char* qlo = ea(8 + l15, 20 * hh + gk);
    const char* qhi = ea(8 + l15, 20 * hh + gk + 4);
    bool m0[4], m1[4];
#pragma unroll
    for (int r = 0; r < 4; ++r) { const int j = 4 * g + r; m0[r] = (j >= l15 + 1) && (j <= l15 + 8); m1[r] = (j <= l15 - 8); }
          const f32x4 z4 = {0.f, 0.f, 0.f, 0.f};
    const s16x4 zz = {0, 0, 0, 0};
    const float ninf = -__builtin_inff();
    bf16x8 kc = cat8(*(const s16x4*)klo, *(const s16x4*)khi);
    s16x4 vc0 = trread(vp0), vc1 = trread(vp1);
#pragma unroll
    for (int y = 0; y < 4; ++y) {
      const bf16x8 kn = cat8(*(const s16x4*)(klo + (y + 1) * TS), *(const s16x4*)(khi + (y + 1) * TS));
      const s16x4 vn0 = trread(vp0 + (y + 1) * TS), vn1 = trread(vp1 + (y + 1) * TS);
      s16x4 a = *(const s16x4*)(qlo + y * TS), b = *(const s16x4*)(qhi + y * TS);
      if (g == 3) a = zz;
      if (g >= 2) b = zz;
      const bf16x8 qf = cat8(a, b);
      f32x4 s0v = mfma16(kc, qf, z4), s1v = mfma16(kn, qf, z4);
#pragma unroll
      for (int r = 0; r < 4; ++r) {
        const bool a0 = (y == 0) ? (m0[r] && !(art == 0 && 4 * g + r < 8)) : m0[r];
        s0v[r] = a0 ? s0v[r] : ninf;
        s1v[r] = m1[r] ? s1v[r] : ninf;
      }
      float mx = fmaxf(__builtin_fmaxf(__builtin_fmaxf(s0v[0], s0v[1]), s0v[2]), __builtin_fmaxf(__builtin_fmaxf(s0v[3], s1v[0]), s1v[1]));
      mx = fmaxf(mx, fmaxf(s1v[2], s1v[3]));
      mx = x16_max(mx);
      const float mc = -mx * QSCALE;
      float p0[4], p1[4];
#pragma unroll
      for (int r = 0; r < 4; ++r) {
        p0[r] = __builtin_amdgcn_exp2f(__builtin_fmaf(s0v[r], QSCALE, mc));
        p1[r] = __builtin_amdgcn_exp2f(__builtin_fmaf(s1v[r], QSCALE, mc));
      }
      const float ls = ((p0[0] + p0[1]) + (p0[2] + p0[3])) + ((p1[0] + p1[1]) + (p1[2] + p1[3]));
      const float inv = __builtin_amdgcn_rcpf(x16_sum(ls));
      bf16x8 pf;
#pragma unroll
      for (int r = 0; r < 4; ++r) { pf[r] = (__bf16)p0[r]; pf[4 + r] = (__bf16)p1[r]; }
      const f32x4 o0 = mfma16(cat8(vc0, vn0), pf, z4);
      const f32x4 o1 = mfma16(cat8(vc1, vn1), pf, z4);
      const int s = art * 128 + 32 * y + 2 * l15 + par;
      char* orow = ws + WS_OA + (size_t)((s >> 5) * 80 + hp * 5) * 512 + (s & 31) * 16;
      const int c0 = 20 * hh + 4 * g, c1 = 20 * hh + 16;
      *(bf16x4*)(orow + (c0 >> 3) * 512 + (c0 & 7) * 2) = pack4(o0[0] * inv, o0[1] * inv, o0[2] * inv, o0[3] * inv);
      if (g == 0) *(bf16x4*)(orow + (c1 >> 3) * 512 + (c1 & 7) * 2) = pack4(o1[0] * inv, o1[1] * inv, o1[2] * inv, o1[3] * inv);
      kc = kn; vc0 = vn0; vc1 = vn1;
    }
    return;
  }
  bf16x8 q1a, q1b; s16x4 q2a, q2b;
  km = x16_max(row16_max(km));
  prep_q(lds + K2_QI + (pr * 2) * 16, lane, km, q1a, q2a);
  prep_q(lds + K2_QI + (pr * 2 + 1) * 16, lane, km, q1b, q2b);
  f32x16 oa = {}, ob = {};
  {
    u32x4 k1, v0, v1 = {0u, 0u, 0u, 0u}; u32x2 k2;
    unsigned a16, a8, n16, n8;
    const unsigned base16 = lbase + kh * 4 * KV_TILE + lane * 16, base8 = lbase + kh * 4 * KV_TILE + lane * 8;
    unsigned b0 = 0, b1 = K2_STAGE, b2 = 2 * K2_STAGE, st, tmp, t2, fv;
    const unsigned fa = lbase + K2_FL, da = lbase + K2_FL + 256 + w * 256 + lane * 4;
    asm volatile(
      "v_add_u32 %[a16], %[b0], %[base16]\n"
      "v_add_u32 %[a8], %[b0], %[base8]\n"
      "ds_read_b128 %[k1], %[a16]\n"
      "ds_read_b64 %[k2], %[a8] offset:1024\n"
      "ds_read_b128 %[v0], %[a16] offset:1536\n"
      "v_mov_b32 v100, 0\n"
      "v_mov_b32 v101, 0\n"
      "v_mov_b32 v102, 0\n"
      "v_mov_b32 v103, 0\n"
      "s_waitcnt lgkmcnt(1)\n"
      "v_mfma_f32_32x32x16_bf16 v[64:79], %[k1], %[q1a], 0\n"
      "v_mfma_f32_32x32x8_bf16 v[64:79], %[k2], %[q2a], v[64:79]\n"
      "s_mov_b32 %[st], 0\n"
      "s_nop 7\n"
      "s_nop 7\n"
      ".p2align 4\n"
      "1:\n"
      "v_add_u32 %[a16], %[b0], %[base16]\n"
      "v_add_u32 %[a8], %[b0], %[base8]\n"
      "v_add_u32 %[n16], %[b1], %[base16]\n"
      "v_add_u32 %[n8], %[b1], %[base8]\n"
      "v_exp_f32 v104, v64\n"
      "v_exp_f32 v105, v65\n"
      "v_mfma_f32_32x32x16_bf16 %[ob], %[v1], v[100:103], %[ob]\n"
      "ds_read_b128 %[v1], %[a16] offset:2560\n"
      "v_exp_f32 v106, v66\n"
      "v_exp_f32 v107, v67\n"
      "v_cvt_pk_bf16_f32 v96, v104, v105\n"
      "v_cvt_pk_bf16_f32 v97, v106, v107\n"
      "v_exp_f32 v108, v68\n"
      "v_exp_f32 v109, v69\n"
      "v_mfma_f32_32x32x16_bf16 v[80:95], %[k1], %[q1b], 0\n"
      "v_exp_f32 v110, v70\n"
      "v_exp_f32 v111, v71\n"
      "v_cvt_pk_bf16_f32 v98, v108, v109\n"
      "v_cvt_pk_bf16_f32 v99, v110, v111\n"
      "v_exp_f32 v104, v72\n"
      "v_exp_f32 v105, v73\n"
      "v_mfma_f32_32x32x8_bf16 v[80:95], %[k2], %[q2b], v[80:95]\n"
      "ds_read_b128 %[k1], %[a16] offset:3584\n"
      "ds_read_b64 %[k2], %[a8] offset:4608\n"
      "v_exp_f32 v106, v74\n"
      "v_exp_f32 v107, v75\n"
      "v_cvt_pk_bf16_f32 v100, v104, v105\n"
      "v_cvt_pk_bf16_f32 v101, v106, v107\n"
      "v_exp_f32 v108, v76\n"
      "v_exp_f32 v109, v77\n"
      "s_waitcnt lgkmcnt(3)\n"
      "v_mfma_f32_32x32x16_bf16 %[oa], %[v0], v[96:99], %[oa]\n"
      "v_exp_f32 v110, v78\n"
      "v_exp_f32 v111, v79\n"
      "v_cvt_pk_bf16_f32 v102, v108, v109\n"
      "v_cvt_pk_bf16_f32 v103, v110, v111\n"
      "v_exp_f32 v104, v80\n"
      "v_exp_f32 v105, v81\n"
      "s_waitcnt lgkmcnt(2)\n"
      "v_mfma_f32_32x32x16_bf16 %[oa], %[v1], v[100:103], %[oa]\n"
      "v_exp_f32 v106, v82\n"
      "v_exp_f32 v107, v83\n"
      "v_cvt_pk_bf16_f32 v96, v104, v105\n"
      "v_cvt_pk_bf16_f32 v97, v106, v107\n"
      "v_exp_f32 v108, v84\n"
      "v_exp_f32 v109, v85\n"
      "s_waitcnt lgkmcnt(1)\n"
      "v_mfma_f32_32x32x16_bf16 v[64:79], %[k1], %[q1a], 0\n"
      "v_exp_f32 v110, v86\n"
      "v_exp_f32 v111, v87\n"
      "v_cvt_pk_bf16_f32 v98, v108, v109\n"
      "v_cvt_pk_bf16_f32 v99, v110, v111\n"
      "v_exp_f32 v104, v88\n"
      "v_exp_f32 v105, v89\n"
      "s_waitcnt lgkmcnt(0)\n"
      "v_mfma_f32_32x32x8_bf16 v[64:79], %[k2], %[q2a], v[64:79]\n"
      "v_exp_f32 v106, v90\n"
      "v_exp_f32 v107, v91\n"
      "v_cvt_pk_bf16_f32 v100, v104, v105\n"
      "v_cvt_pk_bf16_f32 v101, v106, v107\n"
      "v_exp_f32 v108, v92\n"
      "v_exp_f32 v109, v93\n"
      "v_mfma_f32_32x32x16_bf16 %[ob], %[v0], v[96:99], %[ob]\n"
      "ds_read_b128 %[v0], %[a16] offset:5120\n"
      "v_exp_f32 v110, v94\n"
      "v_exp_f32 v111, v95\n"
      "v_cvt_pk_bf16_f32 v102, v108, v109\n"
      "v_cvt_pk_bf16_f32 v103, v110, v111\n"
      "v_exp_f32 v104, v64\n"
      "v_exp_f32 v105, v65\n"
      "v_mfma_f32_32x32x16_bf16 %[ob], %[v1], v[100:103], %[ob]\n"
      "ds_read_b128 %[v1], %[a16] offset:6144\n"
      "v_exp_f32 v106, v66\n"
      "v_exp_f32 v107, v67\n"
      "v_cvt_pk_bf16_f32 v96, v104, v105\n"
      "v_cvt_pk_bf16_f32 v97, v106, v107\n"
      "v_exp_f32 v108, v68\n"
      "v_exp_f32 v109, v69\n"
      "v_mfma_f32_32x32x16_bf16 v[80:95], %[k1], %[q1b], 0\n"
      "v_exp_f32 v110, v70\n"
      "v_exp_f32 v111, v71\n"
      "v_cvt_pk_bf16_f32 v98, v108, v109\n"
      "v_cvt_pk_bf16_f32 v99, v110, v111\n"
      "v_exp_f32 v104, v72\n"
      "v_exp_f32 v105, v73\n"
      "v_mfma_f32_32x32x8_bf16 v[80:95], %[k2], %[q2b], v[80:95]\n"
      "ds_read_b128 %[k1], %[a16] offset:7168\n"
      "ds_read_b64 %[k2], %[a8] offset:8192\n"
      "v_exp_f32 v106, v74\n"
      "v_exp_f32 v107, v75\n"
      "v_cvt_pk_bf16_f32 v100, v104, v105\n"
      "v_cvt_pk_bf16_f32 v101, v106, v107\n"
      "v_exp_f32 v108, v76\n"
      "v_exp_f32 v109, v77\n"
      "s_waitcnt lgkmcnt(3)\n"
      "v_mfma_f32_32x32x16_bf16 %[oa], %[v0], v[96:99], %[oa]\n"
      "v_exp_f32 v110, v78\n"
      "v_exp_f32 v111, v79\n"
      "v_cvt_pk_bf16_f32 v102, v108, v109\n"
      "v_cvt_pk_bf16_f32 v103, v110, v111\n"
      "v_exp_f32 v104, v80\n"
      "v_exp_f32 v105, v81\n"
      "s_waitcnt lgkmcnt(2)\n"
      "v_mfma_f32_32x32x16_bf16 %[oa], %[v1], v[100:103], %[oa]\n"
      "v_exp_f32 v106, v82\n"
      "v_exp_f32 v107, v83\n"
      "v_cvt_pk_bf16_f32 v96, v104, v105\n"
      "v_cvt_pk_bf16_f32 v97, v106, v107\n"
      "v_exp_f32 v108, v84\n"
      "v_exp_f32 v109, v85\n"
      "s_waitcnt lgkmcnt(1)\n"
      "v_mfma_f32_32x32x16_bf16 v[64:79], %[k1], %[q1a], 0\n"
      "v_exp_f32 v110, v86\n"
      "v_exp_f32 v111, v87\n"
      "v_cvt_pk_bf16_f32 v98, v108, v109\n"
      "v_cvt_pk_bf16_f32 v99, v110, v111\n"
      "v_exp_f32 v104, v88\n"
      "v_exp_f32 v105, v89\n"
      "s_waitcnt lgkmcnt(0)\n"
      "v_mfma_f32_32x32x8_bf16 v[64:79], %[k2], %[q2a], v[64:79]\n"
      "v_exp_f32 v106, v90\n"
      "v_exp_f32 v107, v91\n"
      "v_cvt_pk_bf16_f32 v100, v104, v105\n"
      "v_cvt_pk_bf16_f32 v101, v106, v107\n"
      "v_exp_f32 v108, v92\n"
      "v_exp_f32 v109, v93\n"
      "v_mfma_f32_32x32x16_bf16 %[ob], %[v0], v[96:99], %[ob]\n"
      "ds_read_b128 %[v0], %[a16] offset:8704\n"
      "v_exp_f32 v110, v94\n"
      "v_exp_f32 v111, v95\n"
      "v_cvt_pk_bf16_f32 v102, v108, v109\n"
      "v_cvt_pk_bf16_f32 v103, v110, v111\n"
      "v_exp_f32 v104, v64\n"
      "v_exp_f32 v105, v65\n"
      "v_mfma_f32_32x32x16_bf16 %[ob], %[v1], v[100:103], %[ob]\n"
      "ds_read_b128 %[v1], %[a16] offset:9728\n"
      "v_exp_f32 v106, v66\n"
      "v_exp_f32 v107, v67\n"
      "v_cvt_pk_bf16_f32 v96, v104, v105\n"
      "v_cvt_pk_bf16_f32 v97, v106, v107\n"
      "v_exp_f32 v108, v68\n"
      "v_exp_f32 v109, v69\n"
      "v_mfma_f32_32x32x16_bf16 v[80:95], %[k1], %[q1b], 0\n"
      "v_exp_f32 v110, v70\n"
      "v_exp_f32 v111, v71\n"
      "v_cvt_pk_bf16_f32 v98, v108, v109\n"
      "v_cvt_pk_bf16_f32 v99, v110, v111\n"
      "v_exp_f32 v104, v72\n"
      "v_exp_f32 v105, v73\n"
      "v_mfma_f32_32x32x8_bf16 v[80:95], %[k2], %[q2b], v[80:95]\n"
      "ds_read_b128 %[k1], %[a16] offset:10752\n"
      "ds_read_b64 %[k2], %[a8] offset:11776\n"
      "v_exp_f32 v106, v74\n"
      "v_exp_f32 v107, v75\n"
      "v_cvt_pk_bf16_f32 v100, v104, v105\n"
      "v_cvt_pk_bf16_f32 v101, v106, v107\n"
      "v_exp_f32 v108, v76\n"
      "v_exp_f32 v109, v77\n"
      "s_waitcnt lgkmcnt(3)\n"
      "v_mfma_f32_32x32x16_bf16 %[oa], %[v0], v[96:99], %[oa]\n"
      "v_exp_f32 v110, v78\n"
      "v_exp_f32 v111, v79\n"
      "v_cvt_pk_bf16_f32 v102, v108, v109\n"
      "v_cvt_pk_bf16_f32 v103, v110, v111\n"
      "v_exp_f32 v104, v80\n"
      "v_exp_f32 v105, v81\n"
      "s_waitcnt lgkmcnt(2)\n"
      "v_mfma_f32_32x32x16_bf16 %[oa], %[v1], v[100:103], %[oa]\n"
      "v_exp_f32 v106, v82\n"
      "v_exp_f32 v107, v83\n"
      "v_cvt_pk_bf16_f32 v96, v104, v105\n"
      "v_cvt_pk_bf16_f32 v97, v106, v107\n"
      "v_exp_f32 v108, v84\n"
      "v_exp_f32 v109, v85\n"
      "s_waitcnt lgkmcnt(1)\n"
      "v_mfma_f32_32x32x16_bf16 v[64:79], %[k1], %[q1a], 0\n"
      "v_exp_f32 v110, v86\n"
      "v_exp_f32 v111, v87\n"
      "v_cvt_pk_bf16_f32 v98, v108, v109\n"
      "v_cvt_pk_bf16_f32 v99, v110, v111\n"
      "v_exp_f32 v104, v88\n"
      "v_exp_f32 v105, v89\n"
      "s_waitcnt lgkmcnt(0)\n"
      "v_mfma_f32_32x32x8_bf16 v[64:79], %[k2], %[q2a], v[64:79]\n"
      "v_exp_f32 v106, v90\n"
      "v_exp_f32 v107, v91\n"
      "v_cvt_pk_bf16_f32 v100, v104, v105\n"
      "v_cvt_pk_bf16_f32 v101, v106, v107\n"
      "v_exp_f32 v108, v92\n"
      "v_exp_f32 v109, v93\n"
      "v_mfma_f32_32x32x16_bf16 %[ob], %[v0], v[96:99], %[ob]\n"
      "ds_read_b128 %[v0], %[a16] offset:12288\n"
      "v_exp_f32 v110, v94\n"
      "v_exp_f32 v111, v95\n"
      "v_cvt_pk_bf16_f32 v102, v108, v109\n"
      "v_cvt_pk_bf16_f32 v103, v110, v111\n"
      "s_cmp_eq_u32 %[st], 7\n"
      "s_cbranch_scc1 2f\n"
      "s_add_u32 %[tmp], %[st], 2\n"
      "3:\n"
      "ds_read_b32 %[fv], %[fa]\n"
      "s_waitcnt lgkmcnt(0)\n"
      "v_readfirstlane_b32 %[t2], %[fv]\n"
      "s_cmp_ge_u32 %[t2], %[tmp]\n"
      "s_cbranch_scc1 2f\n"
      "s_sleep 1\n"
      "s_branch 3b\n"
      "2:\n"
      "v_exp_f32 v104, v64\n"
      "v_exp_f32 v105, v65\n"
      "v_mfma_f32_32x32x16_bf16 %[ob], %[v1], v[100:103], %[ob]\n"
      "ds_read_b128 %[v1], %[a16] offset:13312\n"
      "v_exp_f32 v106, v66\n"
      "v_exp_f32 v107, v67\n"
      "v_cvt_pk_bf16_f32 v96, v104, v105\n"
      "v_cvt_pk_bf16_f32 v97, v106, v107\n"
      "v_exp_f32 v108, v68\n"
      "v_exp_f32 v109, v69\n"
      "v_mfma_f32_32x32x16_bf16 v[80:95], %[k1], %[q1b], 0\n"
      "v_exp_f32 v110, v70\n"
      "v_exp_f32 v111, v71\n"
      "v_cvt_pk_bf16_f32 v98, v108, v109\n"
      "v_cvt_pk_bf16_f32 v99, v110, v111\n"
      "v_exp_f32 v104, v72\n"
      "v_exp_f32 v105, v73\n"
      "v_mfma_f32_32x32x8_bf16 v[80:95], %[k2], %[q2b], v[80:95]\n"
      "ds_read_b128 %[k1], %[n16]\n"
      "ds_read_b64 %[k2], %[n8] offset:1024\n"
      "v_exp_f32 v106, v74\n"
      "v_exp_f32 v107, v75\n"
      "v_cvt_pk_bf16_f32 v100, v104, v105\n"
      "v_cvt_pk_bf16_f32 v101, v106, v107\n"
      "v_exp_f32 v108, v76\n"
      "v_exp_f32 v109, v77\n"
      "s_waitcnt lgkmcnt(3)\n"
      "v_mfma_f32_32x32x16_bf16 %[oa], %[v0], v[96:99], %[oa]\n"
      "v_exp_f32 v110, v78\n"
      "v_exp_f32 v111, v79\n"
      "v_cvt_pk_bf16_f32 v102, v108, v109\n"
      "v_cvt_pk_bf16_f32 v103, v110, v111\n"
      "v_exp_f32 v104, v80\n"
      "v_exp_f32 v105, v81\n"
      "s_waitcnt lgkmcnt(2)\n"
      "v_mfma_f32_32x32x16_bf16 %[oa], %[v1], v[100:103], %[oa]\n"
      "v_exp_f32 v106, v82\n"
      "v_exp_f32 v107, v83\n"
      "v_cvt_pk_bf16_f32 v96, v104, v105\n"
      "v_cvt_pk_bf16_f32 v97, v106, v107\n"
      "v_exp_f32 v108, v84\n"
      "v_exp_f32 v109, v85\n"
      "s_waitcnt lgkmcnt(1)\n"
      "v_mfma_f32_32x32x16_bf16 v[64:79], %[k1], %[q1a], 0\n"
      "v_exp_f32 v110, v86\n"
      "v_exp_f32 v111, v87\n"
      "v_cvt_pk_bf16_f32 v98, v108, v109\n"
      "v_cvt_pk_bf16_f32 v99, v110, v111\n"
      "v_exp_f32 v104, v88\n"
      "v_exp_f32 v105, v89\n"
      "s_waitcnt lgkmcnt(0)\n"
      "v_mfma_f32_32x32x8_bf16 v[64:79], %[k2], %[q2a], v[64:79]\n"
      "v_exp_f32 v106, v90\n"
      "v_exp_f32 v107, v91\n"
      "v_cvt_pk_bf16_f32 v100, v104, v105\n"
      "v_cvt_pk_bf16_f32 v101, v106, v107\n"
      "v_exp_f32 v108, v92\n"
      "v_exp_f32 v109, v93\n"
      "v_mfma_f32_32x32x16_bf16 %[ob], %[v0], v[96:99], %[ob]\n"
      "ds_read_b128 %[v0], %[n16] offset:1536\n"
      "v_exp_f32 v110, v94\n"
      "v_exp_f32 v111, v95\n"
      "v_cvt_pk_bf16_f32 v102, v108, v109\n"
      "v_cvt_pk_bf16_f32 v103, v110, v111\n"
      "s_add_u32 %[st], %[st], 1\n"
      "v_mov_b32 %[fv], %[st]\n"
      "ds_write_b32 %[da], %[fv]\n"
      "s_mov_b32 %[tmp], %[b0]\n"
      "s_mov_b32 %[b0], %[b1]\n"
      "s_mov_b32 %[b1], %[b2]\n"
      "s_mov_b32 %[b2], %[tmp]\n"
      "s_cmp_lt_u32 %[st], 8\n"
      "s_cbranch_scc1 1b\n"
      "s_nop 1\n"
      "v_mfma_f32_32x32x16_bf16 %[ob], %[v1], v[100:103], %[ob]\n"
      "s_waitcnt lgkmcnt(0)\n"
      "s_nop 15\n"
      : [oa] "+v"(oa), [ob] "+v"(ob), [k1] "=&v"(k1), [k2] "=&v"(k2), [v0] "=&v"(v0), [v1] "+v"(v1),
        [a16] "=&v"(a16), [a8] "=&v"(a8), [n16] "=&v"(n16), [n8] "=&v"(n8),
        [b0] "+s"(b0), [b1] "+s"(b1), [b2] "+s"(b2), [st] "=&s"(st), [tmp] "=&s"(tmp), [t2] "=&s"(t2), [fv] "=&v"(fv)
      : [q1a] "v"(q1a), [q2a] "v"(q2a), [q1b] "v"(q1b), [q2b] "v"(q2b), [base16] "v"(base16), [base8] "v"(base8),
        [fa] "v"(fa), [da] "v"(da)
      : "memory", "scc", "v64", "v65", "v66", "v67", "v68", "v69", "v70", "v71", "v72", "v73", "v74", "v75", "v76", "v77", "v78", "v79", "v80", "v81", "v82", "v83", "v84", "v85", "v86", "v87", "v88", "v89", "v90", "v91", "v92", "v93", "v94", "v95", "v96", "v97", "v98", "v99", "v100", "v101", "v102", "v103", "v104", "v105", "v106", "v107", "v108", "v109", "v110", "v111");
  }
  {
    float* ex = (float*)(lds + K2_EX + (pr * 2 + (kh ^ 1)) * 3072);
    const f32x16& give = kh ? oa : ob;
#pragma unroll
    for (int r4 = 0; r4 < 3; ++r4) {
      f32x4 v = {give[4 * r4], give[4 * r4 + 1], give[4 * r4 + 2], give[4 * r4 + 3]};
      *(f32x4*)(ex + r4 * 256 + lane * 4) = v;
    }
  }
  BAR();
  const int h = lane & 31, hf = lane >> 5;
  {
    const float* ex = (const float*)(lds + K2_EX + (pr * 2 + kh) * 3072);
    const f32x16& mine = kh ? ob : oa;
    float o[12];
#pragma unroll
    for (int r4 = 0; r4 < 3; ++r4) {
      f32x4 v = *(const f32x4*)(ex + r4 * 256 + lane * 4);
#pragma unroll
      for (int r = 0; r < 4; ++r) o[4 * r4 + r] = mine[4 * r4 + r] + v[r];
    }
    const int j = pr * 2 + kh, s = sb + j;
    const float l = x32_sum(hf ? o[8] : 0.f);
    char* oimg = lds + K2_QI + j * 16;
    auto oc = [&](int col) { return (bf16x4*)(oimg + (col >> 3) * 128 + (col & 7) * 2); };
    const bool bad = !(l >= 8.6736174e-19f) || !(l <= 3.0e38f);
    if (__builtin_expect(__any(bad), 0)) {
      const char* qb = ws + WS_QP + (size_t)(s >> 5) * 80 * 512 + (s & 31) * 16;
      float q[20], oo[20];
#pragma unroll
      for (int d = 0; d < 20; ++d) { const int col = 20 * h + d; q[d] = bf2f(*(const __bf16*)(qb + (col >> 3) * 512 + (col & 7) * 2)); oo[d] = 0.f; }
      float m = -3.0e38f, ls = 0.f;
#pragma unroll 1
      for (int kk = 0; kk < S_; ++kk) {
        const char* tb = KVf + (size_t)(kk >> 5) * KV_TILE;
        const int kl = kk & 31;
        float sc = 0.f;
#pragma unroll
        for (int d = 0; d < 16; ++d) sc += q[d] * bf2f(*(const __bf16*)(tb + (kl + 32 * (d >> 3)) * 16 + (d & 7) * 2));
#pragma unroll
        for (int d = 16; d < 20; ++d) sc += q[d] * bf2f(*(const __bf16*)(tb + 1024 + kl * 8 + (d - 16) * 2));
        const float mn = fmaxf(m, sc), al = __builtin_amdgcn_exp2f(m - mn), p = __builtin_amdgcn_exp2f(sc - mn);
        const int sx = kl >> 4, k16 = kl & 15, vh = (k16 >> 2) & 1, jj = (k16 & 3) + 4 * (k16 >> 3);
        ls = ls * al + p;
#pragma unroll
        for (int d = 0; d < 20; ++d) oo[d] = oo[d] * al + p * bf2f(*(const __bf16*)(tb + 1536 + sx * 1024 + (d + 32 * vh) * 16 + jj * 2));
        m = mn;
      }
      const float inv = 1.f / ls;
      if (hf == 0) {
#pragma unroll
        for (int d4 = 0; d4 < 5; ++d4) *oc(20 * h + 4 * d4) = pack4(oo[4 * d4] * inv, oo[4 * d4 + 1] * inv, oo[4 * d4 + 2] * inv, oo[4 * d4 + 3] * inv);
      }
    } else {
      const float inv = __builtin_amdgcn_rcpf(l);
      *oc(20 * h + 4 * hf) = pack4(o[0] * inv, o[1] * inv, o[2] * inv, o[3] * inv);
      *oc(20 * h + 8 + 4 * hf) = pack4(o[4] * inv, o[5] * inv, o[6] * inv, o[7] * inv);
      if (hf == 0) *oc(20 * h + 16) = pack4(o[8] * inv, o[9] * inv, o[10] * inv, o[11] * inv);
    }
  }
  BAR();
  {
    char* op = ws + WS_OB + (size_t)(sb >> 5) * 80 * 512 + (sb & 31) * 16;
#pragma unroll
    for (int ii = 0; ii < 2; ++ii) {
      const int p = w + 8 * ii;
      if (p < 10) *(u32x4*)(op + (8 * p + (lane >> 3)) * 512 + (lane & 7) * 16) = *(const u32x4*)(lds + K2_QI + p * 1024 + lane * 16);
    }
  }
}

constexpr int K3_XB = 24576;

__global__ __launch_bounds__(512, 2) void k3_out(const __bf16* __restrict__ woA, const __bf16* __restrict__ woB,
                                                 char* __restrict__ ws, __bf16* __restrict__ out) {
  __shared__ __attribute__((aligned(16))) char lds[G_NB * G_STAGE];
  const int tid = threadIdx.x, lane = tid & 63, w = __builtin_amdgcn_readfirstlane(tid >> 6);
  const int T = (blockIdx.x & 7) * 28 + (blockIdx.x >> 3);
  if (T >= 220) return;
  const int rt = T / 10, ct = T - rt * 10;
  const unsigned lbase = (unsigned)(size_t)lds;
  const int l15 = lane & 15, g = lane >> 4;
  if (w >= 4) {
    const int lw = w - 4;
    const int rX = ct;
    unsigned aoff[6], woff[4], al[6];
#pragma unroll
    for (int ii = 0; ii < 6; ++ii) {
      int p = lw * 6 + ii + rX; p = p >= 24 ? p - 24 : p;
      const int ln = 8 * p + (lane >> 3);
      int s = rt * 96 + 8 * (ln >> 4); s = s > S_ - 8 ? S_ - 8 : s;
      s += lane & 7;
      aoff[ii] = ((s >> 5) * 80 + (ln & 15)) * 512 + (s & 31) * 16;
      al[ii] = p * 1024;
    }
#pragma unroll
    for (int ii = 0; ii < 4; ++ii) {
      const int k = 8 * (lw * 4 + ii) + (lane >> 3), cp = lane & 7;
      woff[ii] = k * 1280 + ct * 128 + (((cp >> 1) ^ (((k >> 1) & 1) | (((k >> 3) & 1) << 1))) << 5) + ((cp & 1) << 4);
    }
    auto issue = [&](int st, int buf) {
      const int part = st >= 5, kk = st - 5 * part;
      const char* ab = ws + (part ? WS_OB : WS_OA) + kk * 16 * 512;
      const char* wp = (const char*)(part ? woB : woA) + (size_t)kk * 128 * 1280;
      const unsigned lb = lbase + buf * G_STAGE;
      const unsigned wl = lb + K3_XB + lw * 4096;
      dma16(ab, aoff[0], lb + al[0]); dma16(wp, woff[0], wl); dma16(ab, aoff[1], lb + al[1]);
      dma16(ab, aoff[2], lb + al[2]); dma16(wp, woff[1], wl + 1024); dma16(ab, aoff[3], lb + al[3]);
      dma16(wp, woff[2], wl + 2048); dma16(ab, aoff[4], lb + al[4]); dma16(ab, aoff[5], lb + al[5]);
      dma16(wp, woff[3], wl + 3072);
    };
    issue(0, 0); issue(1, 1); issue(2, 2);
    WAIT_BAR(10);
#pragma unroll 1
    for (int st = 0; st < 9; ++st) {
      if (st < 7) { issue(st + 3, (st + 3) & 3); WAIT_BAR(10); }
      else { WAIT_BAR(0); }
    }
  } else {
    const int wm = w >> 1, wn = w & 1;
    const int p4 = lane & 3, q4 = l15 >> 2;
    const int xo0 = ((wm * 6 + (l15 >> 3)) * 16 + g) * 128 + (l15 & 7) * 16;
    const int f = ((q4 >> 1) & 1) | ((g & 1) << 1);
    int wb[2];
#pragma unroll
    for (int j = 0; j < 2; ++j) wb[j] = K3_XB + (8 * g + q4) * 128 + 8 * p4 + 32 * ((2 * wn + j) ^ f);
    float gv[3];
#pragma unroll
    for (int mi = 0; mi < 3; ++mi) {
      int s = rt * 96 + wm * 48 + mi * 16 + l15; s = s > S_ - 1 ? S_ - 1 : s;
      const float gg = ((const float*)(ws + WS_G))[s];
      gv[mi] = gg;
    }
    f32x4 acc[3][2] = {}, accA[3][2];
    bf16x8 xf[4][3], wf[4][2];
    auto ldf = [&](int set, const char* sb, int ks) {
#pragma unroll
      for (int mi = 0; mi < 3; ++mi) xf[set][mi] = *(const bf16x8*)(sb + xo0 + ks * 512 + mi * 4096);
#pragma unroll
      for (int ni = 0; ni < 2; ++ni) {
        const char* pw = sb + wb[ni] + ks * 32 * 128;
        wf[set][ni] = cat8(trread(pw), trread(pw + 4 * 128));
      }
    };
    auto mm = [&](int set) {
#pragma unroll
      for (int mi = 0; mi < 3; ++mi)
#pragma unroll
        for (int ni = 0; ni < 2; ++ni) acc[mi][ni] = mfma16(wf[set][ni], xf[set][mi], acc[mi][ni]);
#pragma unroll
      for (int i = 0; i < 6; ++i) { __builtin_amdgcn_sched_group_barrier(0x8, 1, 0); __builtin_amdgcn_sched_group_barrier(0x100, 1, 0); }
      __builtin_amdgcn_sched_group_barrier(0x100, 1, 0);
      __builtin_amdgcn_sched_barrier(0);
    };
    WAIT_BAR(10);
    ldf(0, lds, 0); ldf(1, lds, 1);
#pragma unroll
    for (int st = 0; st < 10; ++st) {
      const char* sb = lds + (st & 3) * G_STAGE;
      const char* sn = lds + ((st + 1) & 3) * G_STAGE;
      if (st == 5) {
#pragma unroll
        for (int mi = 0; mi < 3; ++mi)
#pragma unroll
          for (int ni = 0; ni < 2; ++ni) { accA[mi][ni] = acc[mi][ni] * gv[mi]; acc[mi][ni] = f32x4{0.f, 0.f, 0.f, 0.f}; }
      }
      ldf(2, sb, 2); mm(0);
      ldf(3, sb, 3); mm(1);
      if (st < 9) ldf(0, sn, 0);
      mm(2);
      if (st < 9) ldf(1, sn, 1);
      mm(3);
      if (st < 9) RAWBAR();
    }
    const int s0 = rt * 96 + wm * 48 + l15;
    char* dst = (char*)out + (size_t)s0 * 1280 + ct * 128 + (wn * 32 + (g & 1) * 16 + (g >> 1) * 8) * 2;
#pragma unroll
    for (int mi = 0; mi < 3; ++mi) {
      const u32x4 v = pair_pack(accA[mi][0] + acc[mi][0] * (1.f - gv[mi]), accA[mi][1] + acc[mi][1] * (1.f - gv[mi]));
      if (s0 + mi * 16 < S_) *(u32x4*)(dst + (size_t)mi * 16 * 1280) = v;
    }
  }
}

extern "C" void kernel_launch(void* const* d_in, const int* in_sizes, int n_in, void* d_out, int out_size, void* d_ws,
                              size_t ws_size, hipStream_t stream) {
  const __bf16* x = (const __bf16*)d_in[0];
  const __bf16* wqkv = (const __bf16*)d_in[1];
  const __bf16* woA = (const __bf16*)d_in[2];
  const __bf16* wq = (const __bf16*)d_in[4];
  const __bf16* wk = (const __bf16*)d_in[5];
  const __bf16* wv = (const __bf16*)d_in[6];
  const __bf16* woB = (const __bf16*)d_in[7];
  const __bf16* gw = (const __bf16*)d_in[8];
  char* ws = (char*)d_ws;
  k1_proj<<<256, 512, 0, stream>>>(x, wqkv, wq, wk, wv, gw, ws);
  k2_attn<<<256, 832, 0, stream>>>(ws, (const char*)woA, (const char*)woB);
  k3_out<<<224, 512, 0, stream>>>(woA, woB, ws, (__bf16*)d_out);
}
```

```cpp
#include <hip/hip_runtime.h>
#include <hip/hip_bf16.h>
#include <stdint.h>

#define DI __device__ __forceinline__
typedef __attribute__((ext_vector_type(8))) __bf16 bf16x8;
typedef __attribute__((ext_vector_type(4))) __bf16 bf16x4;
typedef __attribute__((ext_vector_type(4))) short s16x4;
typedef __attribute__((ext_vector_type(8))) short s16x8;
typedef __attribute__((ext_vector_type(16))) float f32x16;
typedef __attribute__((ext_vector_type(4))) float f32x4;
typedef __attribute__((ext_vector_type(4))) unsigned u32x4;
typedef __attribute__((ext_vector_type(2))) unsigned u32x2;
typedef __attribute__((address_space(3))) s16x4* lds_s16x4_ptr;

constexpr int S_ = 2048, D_ = 640;
constexpr float QSCALE = 0.22360679774997896f * 1.4426950408889634f;

constexpr size_t WS_QP = 0;
constexpr size_t WS_QKV = 2048 * 1280;
constexpr size_t WS_KV = WS_QKV + 2048 * 3840;
constexpr size_t WS_G = 13u << 20;
constexpr size_t WS_KMAX = (13u << 20) + 65536;
constexpr size_t WS_OA = 32u << 20;
constexpr size_t WS_OB = 36u << 20;
constexpr int KV_TILE = 3584;

DI f32x4 mfma16(bf16x8 a, bf16x8 b, f32x4 c) { return __builtin_amdgcn_mfma_f32_16x16x32_bf16(a, b, c, 0, 0, 0); }
DI f32x16 mfma32(bf16x8 a, bf16x8 b, f32x16 c) { return __builtin_amdgcn_mfma_f32_32x32x16_bf16(a, b, c, 0, 0, 0); }
DI s16x4 trread(const char* p) { return __builtin_amdgcn_ds_read_tr16_b64_v4i16((lds_s16x4_ptr)p); }
DI bf16x8 cat8(s16x4 lo, s16x4 hi) { return __builtin_bit_cast(bf16x8, __builtin_shufflevector(lo, hi, 0, 1, 2, 3, 4, 5, 6, 7)); }
DI bf16x4 pack4(float a, float b, float c, float d) { bf16x4 r; r[0] = (__bf16)a; r[1] = (__bf16)b; r[2] = (__bf16)c; r[3] = (__bf16)d; return r; }
DI float bf2f(__bf16 v) { return (float)v; }
DI float x16_max(float x) {
  auto s = __builtin_amdgcn_permlane16_swap(__float_as_uint(x), __float_as_uint(x), false, false);
  x = fmaxf(__uint_as_float(s[0]), __uint_as_float(s[1]));
  auto t = __builtin_amdgcn_permlane32_swap(__float_as_uint(x), __float_as_uint(x), false, false);
  return fmaxf(__uint_as_float(t[0]), __uint_as_float(t[1]));
}
DI float x16_sum(float x) {
  auto s = __builtin_amdgcn_permlane16_swap(__float_as_uint(x), __float_as_uint(x), false, false);
  x = __uint_as_float(s[0]) + __uint_as_float(s[1]);
  auto t = __builtin_amdgcn_permlane32_swap(__float_as_uint(x), __float_as_uint(x), false, false);
  return __uint_as_float(t[0]) + __uint_as_float(t[1]);
}
DI float x32_sum(float x) {
  auto t = __builtin_amdgcn_permlane32_swap(__float_as_uint(x), __float_as_uint(x), false, false);
  return __uint_as_float(t[0]) + __uint_as_float(t[1]);
}
template <int CTRL> DI float dppf(float x) { return __builtin_bit_cast(float, __builtin_amdgcn_mov_dpp(__builtin_bit_cast(int, x), CTRL, 0xf, 0xf, true)); }
DI float row16_max(float x) {
  x = fmaxf(x, dppf<0xB1>(x)); x = fmaxf(x, dppf<0x4E>(x)); x = fmaxf(x, dppf<0x141>(x)); x = fmaxf(x, dppf<0x140>(x));
  return x;
}

DI u32x4 pair_pack(const f32x4& a, const f32x4& b) {
  u32x2 pa = __builtin_bit_cast(u32x2, pack4(a[0], a[1], a[2], a[3])), pb = __builtin_bit_cast(u32x2, pack4(b[0], b[1], b[2], b[3]));
  auto s0 = __builtin_amdgcn_permlane16_swap(pa[0], pb[0], false, false);
  auto s1 = __builtin_amdgcn_permlane16_swap(pa[1], pb[1], false, false);
  return u32x4{s0[0], s1[0], s0[1], s1[1]};
}
DI u32x4 quad_pack(const f32x16& a, int q) {
  u32x2 pa = __builtin_bit_cast(u32x2, pack4(a[4 * q], a[4 * q + 1], a[4 * q + 2], a[4 * q + 3]));
  u32x2 pb = __builtin_bit_cast(u32x2, pack4(a[4 * q + 4], a[4 * q + 5], a[4 * q + 6], a[4 * q + 7]));
  auto s0 = __builtin_amdgcn_permlane32_swap(pa[0], pb[0], false, false);
  auto s1 = __builtin_amdgcn_permlane32_swap(pa[1], pb[1], false, false);
  return u32x4{s0[0], s1[0], s0[1], s1[1]};
}
DI void st16(void* p, u32x4 v) { asm volatile("global_store_dwordx4 %0, %1, off sc1\n\ts_nop 1" :: "v"(p), "v"(v) : "memory"); }
DI void st8(void* p, u32x2 v) { asm volatile("global_store_dwordx2 %0, %1, off sc1" :: "v"(p), "v"(v) : "memory"); }
DI const void* uni_ptr(const void* p) {
  const unsigned long long a = (unsigned long long)p;
  const unsigned lo = __builtin_amdgcn_readfirstlane((unsigned)a), hi = __builtin_amdgcn_readfirstlane((unsigned)(a >> 32));
  return (const void*)(((unsigned long long)hi << 32) | lo);
}
DI void dma16(const void* sbase, unsigned voff, unsigned lds_dst) {
  sbase = uni_ptr(sbase); lds_dst = __builtin_amdgcn_readfirstlane(lds_dst);
  unsigned keep;
  asm volatile("s_mov_b32 %0, m0\n\ts_mov_b32 m0, %3\n\ts_nop 2\n\tglobal_load_lds_dwordx4 %1, %2\n\ts_mov_b32 m0, %0"
               : "=&s"(keep) : "v"(voff), "s"(sbase), "s"(lds_dst) : "memory");
}
DI void dma4(const void* sbase, unsigned voff, unsigned lds_dst) {
  sbase = uni_ptr(sbase); lds_dst = __builtin_amdgcn_readfirstlane(lds_dst);
  unsigned keep;
  asm volatile("s_mov_b32 %0, m0\n\ts_mov_b32 m0, %3\n\ts_nop 2\n\tglobal_load_lds_dword %1, %2\n\ts_mov_b32 m0, %0"
               : "=&s"(keep) : "v"(voff), "s"(sbase), "s"(lds_dst) : "memory");
}
DI void pf_line(const void* gsrc, unsigned lds_dst) {
  lds_dst = __builtin_amdgcn_readfirstlane(lds_dst);
  unsigned keep;
  asm volatile("s_mov_b32 %0, m0\n\ts_mov_b32 m0, %2\n\ts_nop 0\n\tglobal_load_lds_dword %1, off\n\ts_mov_b32 m0, %0"
               : "=&s"(keep) : "v"(gsrc), "s"(lds_dst) : "memory");
}
#define WAIT_BAR(n) asm volatile("s_waitcnt vmcnt(" #n ") lgkmcnt(0)\n\ts_barrier" ::: "memory")
#define BAR() asm volatile("s_waitcnt lgkmcnt(0)\n\ts_barrier" ::: "memory")
#define RAWBAR() asm volatile("s_barrier" ::: "memory")

constexpr int G_STAGE = 40960, G_NB = 4;
constexpr int K1_XB = 16384;

DI void k1_kv_block(char* lds, int kvb, int w, int lane, const __bf16* x, const __bf16* wk, const __bf16* wv,
                    const __bf16* gw, char* KVf, float* gout, float* kmax2) {
  const unsigned lbase = (unsigned)(size_t)lds;
  const int tid = w * 64 + lane, l15 = lane & 15, g = lane >> 4;
  constexpr int WK = 81920, WV = WK + 25600, GW = WV + 25600;
  {
    const char* xb = (const char*)x + (size_t)kvb * 64 * 1280;
#pragma unroll 2
    for (int p = w; p < 80; p += 8) {
      const int q = p * 64 + lane, row = q / 80, cp = q - row * 80;
      dma16(xb, row * 1280 + (((cp & ~7) | ((cp ^ row) & 7)) << 4), lbase + p * 1024);
    }
    for (int p = w; p < 50; p += 8) {
      const char* src = (p < 25) ? (const char*)wk + p * 1024 : (const char*)wv + (p - 25) * 1024;
      dma16(src, lane * 16, lbase + WK + p * 1024);
    }
    for (int k = tid; k < 640; k += 512) {
      bf16x4 v; v[0] = gw[k]; v[1] = (__bf16)0.f; v[2] = (__bf16)0.f; v[3] = (__bf16)0.f;
      *(bf16x4*)(lds + GW + k * 40) = v;
    }
  }
  WAIT_BAR(0);
  f32x4 acc[3] = {};
  const int row = (w & 3) * 16 + l15;
  if (w < 4) {
    const int p4 = lane & 3, q4 = l15 >> 2;
    int wa[3];
#pragma unroll
    for (int ni = 0; ni < 3; ++ni) {
      const int cg = 4 * ni + p4;
      wa[ni] = (cg < 5 ? WK + cg * 8 : (cg < 10 ? WV + (cg - 5) * 8 : GW)) + (8 * g + q4) * 40;
    }
    const int xo = row * 1280 + ((g ^ (row & 7)) << 4);
#pragma unroll
    for (int ks = 0; ks < 20; ++ks) {
      const bf16x8 xf = *(const bf16x8*)(lds + (xo ^ ((ks & 1) * 64)) + (ks >> 1) * 128);
#pragma unroll
      for (int ni = 0; ni < 3; ++ni) {
        const char* pw = lds + wa[ni] + ks * 32 * 40;
        acc[ni] = mfma16(cat8(trread(pw), trread(pw + 160)), xf, acc[ni]);
      }
    }
  }
  BAR();
  if (w < 4) {
#pragma unroll
    for (int ni = 0; ni < 3; ++ni) {
      const int cg = 4 * ni + g;
      const bf16x4 v = pack4(acc[ni][0], acc[ni][1], acc[ni][2], acc[ni][3]);
      if (cg < 5) *(bf16x4*)(lds + row * 96 + cg * 8) = v;
      else if (cg < 10) *(bf16x4*)(lds + row * 96 + 48 + (cg - 5) * 8) = v;
      else if (cg == 10) gout[kvb * 64 + row] = 1.f / (1.f + __expf(-acc[ni][0]));
    }
  }
  BAR();
  if (w >= 2) return;
  const char* T = lds + w * 32 * 96;
  const int t = kvb * 2 + w;
  char* dst = KVf + (size_t)t * KV_TILE;
  const int key = lane & 31, hf = lane >> 5;
  const u32x4 k1 = *(const u32x4*)(T + key * 96 + hf * 16);
  u32x2 k2 = *(const u32x2*)(T + key * 96 + 32);
  {
    const bf16x8 a = __builtin_bit_cast(bf16x8, k1); const bf16x4 b = __builtin_bit_cast(bf16x4, k2);
    float n = 0.f;
#pragma unroll
    for (int j = 0; j < 8; ++j) n += bf2f(a[j]) * bf2f(a[j]);
    float n3 = 0.f;
#pragma unroll
    for (int j = 0; j < 4; ++j) n3 += bf2f(b[j]) * bf2f(b[j]);
    n = x32_sum(n) + n3;
    n = x16_max(row16_max(n));
    if (lane == 0) kmax2[t] = n;
  }
  st16(dst + lane * 16, k1);
  if (hf) { k2[0] = 0x3F80u; k2[1] = 0u; }
  st8(dst + 1024 + lane * 8, k2);
#pragma unroll
  for (int s = 0; s < 2; ++s) {
    s16x8 v;
#pragma unroll
    for (int j = 0; j < 8; ++j) {
      const int kk = 16 * s + 8 * (j >> 2) + 4 * hf + (j & 3);
      const short e = *(const short*)(T + kk * 96 + 48 + (key < 20 ? key : 0) * 2);
      v[j] = key < 20 ? e : (key == 20 ? (short)0x3F80 : (short)0);
    }
    st16(dst + 1536 + s * 1024 + lane * 16, __builtin_bit_cast(u32x4, v));
  }
}

__global__ __launch_bounds__(512, 2) void k1_proj(const __bf16* __restrict__ x, const __bf16* __restrict__ wqkv,
                                                  const __bf16* __restrict__ wq, const __bf16* __restrict__ wk,
                                                  const __bf16* __restrict__ wv, const __bf16* __restrict__ gw,
                                                  char* __restrict__ ws) {
  __shared__ __attribute__((aligned(16))) char lds[G_NB * G_STAGE];
  const int tid = threadIdx.x, lane = tid & 63, w = __builtin_amdgcn_readfirstlane(tid >> 6);
  const int b = blockIdx.x, xcd = b & 7, idx = b >> 3;
  if (__builtin_expect(idx >= 28, 0)) {
    k1_kv_block(lds, xcd * 4 + (idx - 28), w, lane, x, wk, wv, gw, ws + WS_KV, (float*)(ws + WS_G), (float*)(ws + WS_KMAX));
    return;
  }
  const int rl = (idx * 37) >> 8, cl7 = idx - rl * 7;
  const int rt = 4 * (xcd >> 1) + rl, ct = 7 * (xcd & 1) + cl7;
  const unsigned lbase = (unsigned)(size_t)lds;
  const int l15 = lane & 15, g = lane >> 4;
  if (__builtin_expect(w >= 4, 1)) {
    const int lw = w - 4;
    const int rX = cl7, rW = (0x5320 >> (4 * rl)) & 15;
    const char* xb0 = (const char*)x + (size_t)rt * 128 * 1280;
    const unsigned xe = (lane >> 3) * 1280 + (((lane & 7) ^ (lane >> 4)) << 4);
    const unsigned xv0 = xe ^ ((rX & 1) << 6), xv1 = xv0 ^ 64;
    const bool isq = ct >= 10;
    const unsigned wstride = isq ? 1280u : 3840u;
    const char* wb0 = (isq ? (const char*)wq + (ct - 10) * 384 : (const char*)wqkv + ct * 384);
    const unsigned wv_ = (lane >> 3) * wstride + ((((lane & 7) >> 1) ^ ((lane >> 3) & 2)) << 5) + ((lane & 1) << 4);
    auto issue = [&](int st, int buf) {
      const char* xb = xb0 + st * 128;
      const char* wp = wb0 + (size_t)st * 64 * wstride;
      const unsigned lb = lbase + buf * G_STAGE;
      auto xp = [&](int ii) { const int p = (lw * 4 + ii + rX) & 15; dma16(xb + p * 8 * 1280, (ii & 1) ? xv1 : xv0, lb + p * 1024); };
      auto wpc = [&](int ii) {
        int p = lw * 6 + ii + rW; p = p >= 24 ? p - 24 : p;
        const int ko = (p * 43) >> 7, th = p - ko * 3;
        dma16(wp + (size_t)ko * 8 * wstride + (ct == 13 ? 0 : th * 128), wv_, lb + K1_XB + p * 1024);
      };
      xp(0); wpc(0); wpc(1); xp(1); wpc(2); xp(2); wpc(3); wpc(4); xp(3); wpc(5);
    };
    issue(0, 0); issue(1, 1);
    WAIT_BAR(10);
    issue(2, 2);
    WAIT_BAR(10);
#pragma unroll 1
    for (int st = 0; st < 9; ++st) {
      if (st < 7) { issue(st + 3, (st + 3) & 3); WAIT_BAR(10); }
      else { WAIT_BAR(0); }
    }
  } else {
    {
      const int bi = rt * 14 + ct;
      int li = w * 64 + lane; li = li > 205 ? 205 : li;
      int ln = bi * 206 + li; ln = ln > 46079 ? 46079 : ln;
      const char* src = ln < 20480 ? (const char*)x + (size_t)ln * 128
                                   : (ln < 39680 ? (const char*)wqkv + (size_t)(ln - 20480) * 128 : (const char*)wq + (size_t)(ln - 39680) * 128);
      pf_line(src, lbase + 3 * G_STAGE + w * 256);
    }
    const int wm = w >> 1, wn = w & 1;
    const int l31 = lane & 31, h = lane >> 5, p4 = lane & 3, q4 = l15 >> 2;
    const int xo0 = (wm * 64 + l31) * 128 + ((h ^ ((l31 >> 1) & 7)) << 4);
    int wo[3];
    {
      const int cA = 32 * ((g & 1) ^ (q4 & 2)), wbase = K1_XB + h * 3072 + q4 * 128 + 8 * p4;
      wo[0] = wbase + (wn ? 1024 + (cA ^ 64) : cA);
      wo[1] = wbase + (wn ? 2048 + cA : (cA ^ 64));
      wo[2] = wbase + (wn ? 2048 + (cA ^ 64) : 1024 + cA);
    }
    f32x16 acc[2][3] = {};
    bf16x8 xa[2], wa[3], xb[2], wc[3];
    auto ldf = [&](bf16x8 (&xf)[2], bf16x8 (&wf)[3], const char* sb, int ks) {
#pragma unroll
      for (int mi = 0; mi < 2; ++mi) xf[mi] = *(const bf16x8*)(sb + (xo0 ^ (ks * 32)) + mi * 4096);
#pragma unroll
      for (int ni = 0; ni < 3; ++ni) {
        const char* pw = sb + wo[ni] + ks * 6144;
        wf[ni] = cat8(trread(pw), trread(pw + 512));
      }
    };
    auto mm = [&](const bf16x8 (&xf)[2], const bf16x8 (&wf)[3]) {
#pragma unroll
      for (int mi = 0; mi < 2; ++mi)
#pragma unroll
        for (int ni = 0; ni < 3; ++ni) acc[mi][ni] = mfma32(wf[ni], xf[mi], acc[mi][ni]);
#pragma unroll
      for (int i = 0; i < 4; ++i) { __builtin_amdgcn_sched_group_barrier(0x8, 1, 0); __builtin_amdgcn_sched_group_barrier(0x100, 2, 0); }
      __builtin_amdgcn_sched_group_barrier(0x8, 2, 0);
      __builtin_amdgcn_sched_barrier(0);
    };
    RAWBAR();
    ldf(xa, wa, lds, 0);
#pragma unroll
    for (int st = 0; st < 10; ++st) {
      const char* sb = lds + (st & 3) * G_STAGE;
      const char* sn = lds + ((st + 1) & 3) * G_STAGE;
      ldf(xb, wc, sb, 1); mm(xa, wa);
      ldf(xa, wa, sb, 2); mm(xb, wc);
      ldf(xb, wc, sb, 3); mm(xa, wa);
      if (st == 0) asm volatile("s_waitcnt vmcnt(0)\n\ts_barrier" ::: "memory");
      if (st < 9) ldf(xa, wa, sn, 0);
      mm(xb, wc);
      if (st < 9) RAWBAR();
    }
    char* dst; int no, oc0;
    if (ct >= 10) {
      dst = ws + WS_QP; no = 80; oc0 = (ct - 10) * 24;
#pragma unroll
      for (int mi = 0; mi < 2; ++mi)
#pragma unroll
        for (int ni = 0; ni < 3; ++ni) acc[mi][ni] *= QSCALE;
    } else { dst = ws + WS_QKV; no = 240; oc0 = ct * 24; }
    oc0 += wn * 12;
    dst += (size_t)((rt * 4 + wm * 2) * no) * 512 + lane * 16;
#pragma unroll
    for (int mi = 0; mi < 2; ++mi)
#pragma unroll
      for (int ni = 0; ni < 3; ++ni)
#pragma unroll
        for (int qq = 0; qq < 2; ++qq) {
          const u32x4 v = quad_pack(acc[mi][ni], 2 * qq);
          const int oc = oc0 + 4 * ni + 2 * qq;
          if (oc < no) st16(dst + (size_t)(mi * no + oc) * 512, v);
        }
  }
}

constexpr int K2_STAGE = 8 * KV_TILE;
constexpr unsigned A_START = 0;
constexpr int K2_RA = 3 * K2_STAGE;
constexpr int K2_QI = K2_RA + 38 * 1024;
constexpr int K2_EX = K2_QI + 10240;
constexpr int K2_FL = K2_EX + 8 * 3072;
constexpr int K2_SCR = K2_FL + 2304;

DI void prep_q(const char* qi, int lane, float km, bf16x8& q1, s16x4& q2) {
  const int h = lane & 31, hf = lane >> 5;
  auto qa = [&](int col) { return *(const u32x2*)(qi + (col >> 3) * 128 + (col & 7) * 2); };
  const u32x2 lo = qa(20 * h + 8 * hf), hi = qa(20 * h + 8 * hf + 4), tl = qa(20 * h + 16);
  u32x4 qq = {lo[0], lo[1], hi[0], hi[1]};
  q1 = __builtin_bit_cast(bf16x8, qq);
  bf16x4 t4 = __builtin_bit_cast(bf16x4, tl);
  float n2 = 0.f;
#pragma unroll
  for (int j = 0; j < 8; ++j) { float v = bf2f(q1[j]); n2 += v * v; }
  float n3 = 0.f;
#pragma unroll
  for (int j = 0; j < 4; ++j) { float v = bf2f(t4[j]); n3 += v * v; }
  n2 = x32_sum(n2) + n3;
  const float off = __builtin_sqrtf(n2 * km) * 1.01f - 60.f;
  unsigned ob = __float_as_uint(off);
  ob = (off > 0.f) ? ((ob + 0xFFFFu) & 0xFFFF0000u) : (ob & 0xFFFF0000u);
  u32x2 t2 = tl;
  if (hf) { t2[0] = (ob >> 16) ^ 0x8000u; t2[1] = 0u; }
  q2 = __builtin_bit_cast(s16x4, t2);
}

__global__ __launch_bounds__(832, 4) void k2_attn(char* __restrict__ ws, const char* __restrict__ woA, const char* __restrict__ woB) {
  __shared__ __attribute__((aligned(16))) char lds[K2_SCR + 256];
  const int tid = threadIdx.x, lane = tid & 63, w = __builtin_amdgcn_readfirstlane(tid >> 6);
  const unsigned lbase = (unsigned)(size_t)lds;
  const char* KVf = ws + WS_KV;
  volatile unsigned* fl = (volatile unsigned*)(lds + K2_FL);
  const int pr = w & 3, kh = (w >> 2) & 1;
  const int sb = blockIdx.x * 8;
  const int hp = blockIdx.x & 15, art = blockIdx.x >> 4;
  if (w < 8) {
    const char* qp = ws + WS_QP + (size_t)(sb >> 5) * 80 * 512 + (sb & 31) * 16;
#pragma unroll
    for (int ii = 0; ii < 2; ++ii) {
      int p = w + 8 * ii; p = p > 9 ? 9 : p;
      dma16(qp, (8 * p + (lane >> 3)) * 512 + (lane & 7) * 16, lbase + K2_QI + p * 1024);
    }
    const int wq = w > 6 ? 6 : w;
#pragma unroll
    for (int k = 0; k < 4; ++k) dma16(KVf + wq * 4096 + k * 1024, lane * 16, lbase + wq * 4096 + k * 1024);
  } else {
    for (int i = tid - 512; i < 576; i += 320) fl[i] = i == 0 ? 1u : 0u;
  }
  float km = 0.f;
  if (w < 8) km = ((const float*)(ws + WS_KMAX))[lane];
  WAIT_BAR(0);
  if (w == 8) {
    __builtin_amdgcn_s_setprio(3);
    const unsigned dv = lane * 16;
#pragma unroll 1
    for (int st = 1; st < 8; ++st) {
      if (st >= 3) {
        while (true) {
          const unsigned d = fl[64 + 64 * (lane & 7)];
          if (__all(d >= (unsigned)(st - 2))) break;
          __builtin_amdgcn_s_sleep(2);
        }
      }
      const char* src = KVf + st * K2_STAGE;
      const unsigned ld = lbase + (st % 3) * K2_STAGE;
#pragma unroll 4
      for (int k = 0; k < 28; ++k) dma16(src + k * 1024, dv, ld + k * 1024);
      asm volatile("s_waitcnt vmcnt(0)" ::: "memory");
      fl[0] = st + 1;
      if (st == 4) {
        const int grp = blockIdx.x >> 3;
#pragma unroll
        for (int k = 0; k < 7; ++k) {
          int ln = k * 64 + lane; ln = ln > 399 ? 399 : ln;
          dma4(grp >= 16 ? woB : woA, ((grp & 15) * 400 + ln) * 128, lbase + K2_SCR);
        }
      }
    }
    return;
  }
  if (w > 8) {
    {
      const char* rap = ws + WS_QKV + ((long)art * 4 - 1) * 240 * 512 + hp * 5 * 512;
      unsigned rp[3];
#pragma unroll
      for (int t = 0; t < 3; ++t) { const int r = 16 + 64 * t + lane; rp[t] = (r >> 5) * 240 * 512 + (r & 31) * 16; }
#pragma unroll 1
      for (int co = w - 9; co < 15; co += 4) {
        const int seg = (co * 13) >> 6;
        const char* src = rap + (seg * 75 + co) * 512;
        const unsigned ld = lbase + K2_RA + co * 2560;
        dma16(src, rp[0], ld); dma16(src, rp[1], ld + 1024);
        if (lane < 32) dma16(src, rp[2], ld + 2048);
      }
      asm volatile("s_waitcnt vmcnt(0)" ::: "memory");
      if (lane == 0) __hip_atomic_fetch_add((unsigned*)(lds + K2_FL + 4), 1u, __ATOMIC_RELAXED, __HIP_MEMORY_SCOPE_WORKGROUP);
    }
    while (fl[64 + 64 * (w & 3)] < A_START) __builtin_amdgcn_s_sleep(16);
    while (fl[1] < 4u) __builtin_amdgcn_s_sleep(8);
    const int l15 = lane & 15, g = lane >> 4, p4 = lane & 3, q4 = l15 >> 2;
    const int hh = w & 1, par = ((w - 9) >> 1) & 1;
    constexpr int TS = 4 * 128;
    const char* RA = lds + K2_RA;
    auto ea = [&](int u, int cc) { const int i = 2 * u + par; return RA + ((cc >> 3) * 20 + (i >> 3)) * 128 + (i & 7) * 16 + (cc & 7) * 2; };
    const int gk = g < 3 ? 8 * g : 0;
    const char* klo = ea(l15, 40 + 20 * hh + gk);
    const char* khi = ea(l15, 40 + 20 * hh + gk + 4);
    const char* vp0 = ea(4 * g + q4, 80 + 20 * hh + 4 * p4);
    const char* vp1 = ea(4 * g + q4, 80 + 20 * hh + 16 + (hh == 1 && p4 > 0 ? 0 : 4 * p4));
    const char* qlo = ea(8 + l15, 20 * hh + gk);
    const char* qhi = ea(8 + l15, 20 * hh + gk + 4);
    bool m0[4], m1[4];
#pragma unroll
    for (int r = 0; r < 4; ++r) { const int j = 4 * g + r; m0[r] = (j >= l15 + 1) && (j <= l15 + 8); m1[r] = (j <= l15 - 8); }
          const f32x4 z4 = {0.f, 0.f, 0.f, 0.f};
    const s16x4 zz = {0, 0, 0, 0};
    const float ninf = -__builtin_inff();
    bf16x8 kc = cat8(*(const s16x4*)klo, *(const s16x4*)khi);
    s16x4 vc0 = trread(vp0), vc1 = trread(vp1);
#pragma unroll
    for (int y = 0; y < 4; ++y) {
      const bf16x8 kn = cat8(*(const s16x4*)(klo + (y + 1) * TS), *(const s16x4*)(khi + (y + 1) * TS));
      const s16x4 vn0 = trread(vp0 + (y + 1) * TS), vn1 = trread(vp1 + (y + 1) * TS);
      s16x4 a = *(const s16x4*)(qlo + y * TS), b = *(const s16x4*)(qhi + y * TS);
      if (g == 3) a = zz;
      if (g >= 2) b = zz;
      const bf16x8 qf = cat8(a, b);
      f32x4 s0v = mfma16(kc, qf, z4), s1v = mfma16(kn, qf, z4);
#pragma unroll
      for (int r = 0; r < 4; ++r) {
        const bool a0 = (y == 0) ? (m0[r] && !(art == 0 && 4 * g + r < 8)) : m0[r];
        s0v[r] = a0 ? s0v[r] : ninf;
        s1v[r] = m1[r] ? s1v[r] : ninf;
      }
      float mx = fmaxf(__builtin_fmaxf(__builtin_fmaxf(s0v[0], s0v[1]), s0v[2]), __builtin_fmaxf(__builtin_fmaxf(s0v[3], s1v[0]), s1v[1]));
      mx = fmaxf(mx, fmaxf(s1v[2], s1v[3]));
      mx = x16_max(mx);
      const float mc = -mx * QSCALE;
      float p0[4], p1[4];
#pragma unroll
      for (int r = 0; r < 4; ++r) {
        p0[r] = __builtin_amdgcn_exp2f(__builtin_fmaf(s0v[r], QSCALE, mc));
        p1[r] = __builtin_amdgcn_exp2f(__builtin_fmaf(s1v[r], QSCALE, mc));
      }
      const float ls = ((p0[0] + p0[1]) + (p0[2] + p0[3])) + ((p1[0] + p1[1]) + (p1[2] + p1[3]));
      const float inv = __builtin_amdgcn_rcpf(x16_sum(ls));
      bf16x8 pf;
#pragma unroll
      for (int r = 0; r < 4; ++r) { pf[r] = (__bf16)p0[r]; pf[4 + r] = (__bf16)p1[r]; }
      const f32x4 o0 = mfma16(cat8(vc0, vn0), pf, z4);
      const f32x4 o1 = mfma16(cat8(vc1, vn1), pf, z4);
      const int s = art * 128 + 32 * y + 2 * l15 + par;
      char* orow = ws + WS_OA + (size_t)s * 1280 + (2 * hp + hh) * 40;
      st8(orow + g * 8, __builtin_bit_cast(u32x2, pack4(o0[0] * inv, o0[1] * inv, o0[2] * inv, o0[3] * inv)));
      if (g == 0) st8(orow + 32, __builtin_bit_cast(u32x2, pack4(o1[0] * inv, o1[1] * inv, o1[2] * inv, o1[3] * inv)));
      kc = kn; vc0 = vn0; vc1 = vn1;
    }
    return;
  }
  bf16x8 q1a, q1b; s16x4 q2a, q2b;
  km = x16_max(row16_max(km));
  prep_q(lds + K2_QI + (pr * 2) * 16, lane, km, q1a, q2a);
  prep_q(lds + K2_QI + (pr * 2 + 1) * 16, lane, km, q1b, q2b);
  f32x16 oa = {}, ob = {};
  {
    u32x4 k1, v0, v1 = {0u, 0u, 0u, 0u}; u32x2 k2;
    unsigned a16, a8, n16, n8;
    const unsigned base16 = lbase + kh * 4 * KV_TILE + lane * 16, base8 = lbase + kh * 4 * KV_TILE + lane * 8;
    unsigned b0 = 0, b1 = K2_STAGE, b2 = 2 * K2_STAGE, st, tmp, t2, fv;
    const unsigned fa = lbase + K2_FL, da = lbase + K2_FL + 256 + w * 256 + lane * 4;
    asm volatile(
      "v_add_u32 %[a16], %[b0], %[base16]\n"
      "v_add_u32 %[a8], %[b0], %[base8]\n"
      "ds_read_b128 %[k1], %[a16]\n"
      "ds_read_b64 %[k2], %[a8] offset:1024\n"
      "ds_read_b128 %[v0], %[a16] offset:1536\n"
      "v_mov_b32 v100, 0\n"
      "v_mov_b32 v101, 0\n"
      "v_mov_b32 v102, 0\n"
      "v_mov_b32 v103, 0\n"
      "s_waitcnt lgkmcnt(1)\n"
      "v_mfma_f32_32x32x16_bf16 v[64:79], %[k1], %[q1a], 0\n"
      "v_mfma_f32_32x32x8_bf16 v[64:79], %[k2], %[q2a], v[64:79]\n"
      "s_mov_b32 %[st], 0\n"
      "s_nop 7\n"
      "s_nop 7\n"
      ".p2align 4\n"
      "1:\n"
      "v_add_u32 %[a16], %[b0], %[base16]\n"
      "v_add_u32 %[a8], %[b0], %[base8]\n"
      "v_add_u32 %[n16], %[b1], %[base16]\n"
      "v_add_u32 %[n8], %[b1], %[base8]\n"
      "v_exp_f32 v104, v64\n"
      "v_exp_f32 v105, v65\n"
      "v_mfma_f32_32x32x16_bf16 %[ob], %[v1], v[100:103], %[ob]\n"
      "ds_read_b128 %[v1], %[a16] offset:2560\n"
      "v_exp_f32 v106, v66\n"
      "v_exp_f32 v107, v67\n"
      "v_cvt_pk_bf16_f32 v96, v104, v105\n"
      "v_cvt_pk_bf16_f32 v97, v106, v107\n"
      "v_exp_f32 v108, v68\n"
      "v_exp_f32 v109, v69\n"
      "v_mfma_f32_32x32x16_bf16 v[80:95], %[k1], %[q1b], 0\n"
      "v_exp_f32 v110, v70\n"
      "v_exp_f32 v111, v71\n"
      "v_cvt_pk_bf16_f32 v98, v108, v109\n"
      "v_cvt_pk_bf16_f32 v99, v110, v111\n"
      "v_exp_f32 v104, v72\n"
      "v_exp_f32 v105, v73\n"
      "v_mfma_f32_32x32x8_bf16 v[80:95], %[k2], %[q2b], v[80:95]\n"
      "ds_read_b128 %[k1], %[a16] offset:3584\n"
      "ds_read_b64 %[k2], %[a8] offset:4608\n"
      "v_exp_f32 v106, v74\n"
      "v_exp_f32 v107, v75\n"
      "v_cvt_pk_bf16_f32 v100, v104, v105\n"
      "v_cvt_pk_bf16_f32 v101, v106, v107\n"
      "v_exp_f32 v108, v76\n"
      "v_exp_f32 v109, v77\n"
      "s_waitcnt lgkmcnt(3)\n"
      "v_mfma_f32_32x32x16_bf16 %[oa], %[v0], v[96:99], %[oa]\n"
      "v_exp_f32 v110, v78\n"
      "v_exp_f32 v111, v79\n"
      "v_cvt_pk_bf16_f32 v102, v108, v109\n"
      "v_cvt_pk_bf16_f32 v103, v110, v111\n"
      "v_exp_f32 v104, v80\n"
      "v_exp_f32 v105, v81\n"
      "s_waitcnt lgkmcnt(2)\n"
      "v_mfma_f32_32x32x16_bf16 %[oa], %[v1], v[100:103], %[oa]\n"
      "v_exp_f32 v106, v82\n"
      "v_exp_f32 v107, v83\n"
      "v_cvt_pk_bf16_f32 v96, v104, v105\n"
      "v_cvt_pk_bf16_f32 v97, v106, v107\n"
      "v_exp_f32 v108, v84\n"
      "v_exp_f32 v109, v85\n"
      "s_waitcnt lgkmcnt(1)\n"
      "v_mfma_f32_32x32x16_bf16 v[64:79], %[k1], %[q1a], 0\n"
      "v_exp_f32 v110, v86\n"
      "v_exp_f32 v111, v87\n"
      "v_cvt_pk_bf16_f32 v98, v108, v109\n"
      "v_cvt_pk_bf16_f32 v99, v110, v111\n"
      "v_exp_f32 v104, v88\n"
      "v_exp_f32 v105, v89\n"
      "s_waitcnt lgkmcnt(0)\n"
      "v_mfma_f32_32x32x8_bf16 v[64:79], %[k2], %[q2a], v[64:79]\n"
      "v_exp_f32 v106, v90\n"
      "v_exp_f32 v107, v91\n"
      "v_cvt_pk_bf16_f32 v100, v104, v105\n"
      "v_cvt_pk_bf16_f32 v101, v106, v107\n"
      "v_exp_f32 v108, v92\n"
      "v_exp_f32 v109, v93\n"
      "v_mfma_f32_32x32x16_bf16 %[ob], %[v0], v[96:99], %[ob]\n"
      "ds_read_b128 %[v0], %[a16] offset:5120\n"
      "v_exp_f32 v110, v94\n"
      "v_exp_f32 v111, v95\n"
      "v_cvt_pk_bf16_f32 v102, v108, v109\n"
      "v_cvt_pk_bf16_f32 v103, v110, v111\n"
      "v_exp_f32 v104, v64\n"
      "v_exp_f32 v105, v65\n"
      "v_mfma_f32_32x32x16_bf16 %[ob], %[v1], v[100:103], %[ob]\n"
      "ds_read_b128 %[v1], %[a16] offset:6144\n"
      "v_exp_f32 v106, v66\n"
      "v_exp_f32 v107, v67\n"
      "v_cvt_pk_bf16_f32 v96, v104, v105\n"
      "v_cvt_pk_bf16_f32 v97, v106, v107\n"
      "v_exp_f32 v108, v68\n"
      "v_exp_f32 v109, v69\n"
      "v_mfma_f32_32x32x16_bf16 v[80:95], %[k1], %[q1b], 0\n"
      "v_exp_f32 v110, v70\n"
      "v_exp_f32 v111, v71\n"
      "v_cvt_pk_bf16_f32 v98, v108, v109\n"
      "v_cvt_pk_bf16_f32 v99, v110, v111\n"
      "v_exp_f32 v104, v72\n"
      "v_exp_f32 v105, v73\n"
      "v_mfma_f32_32x32x8_bf16 v[80:95], %[k2], %[q2b], v[80:95]\n"
      "ds_read_b128 %[k1], %[a16] offset:7168\n"
      "ds_read_b64 %[k2], %[a8] offset:8192\n"
      "v_exp_f32 v106, v74\n"
      "v_exp_f32 v107, v75\n"
      "v_cvt_pk_bf16_f32 v100, v104, v105\n"
      "v_cvt_pk_bf16_f32 v101, v106, v107\n"
      "v_exp_f32 v108, v76\n"
      "v_exp_f32 v109, v77\n"
      "s_waitcnt lgkmcnt(3)\n"
      "v_mfma_f32_32x32x16_bf16 %[oa], %[v0], v[96:99], %[oa]\n"
      "v_exp_f32 v110, v78\n"
      "v_exp_f32 v111, v79\n"
      "v_cvt_pk_bf16_f32 v102, v108, v109\n"
      "v_cvt_pk_bf16_f32 v103, v110, v111\n"
      "v_exp_f32 v104, v80\n"
      "v_exp_f32 v105, v81\n"
      "s_waitcnt lgkmcnt(2)\n"
      "v_mfma_f32_32x32x16_bf16 %[oa], %[v1], v[100:103], %[oa]\n"
      "v_exp_f32 v106, v82\n"
      "v_exp_f32 v107, v83\n"
      "v_cvt_pk_bf16_f32 v96, v104, v105\n"
      "v_cvt_pk_bf16_f32 v97, v106, v107\n"
      "v_exp_f32 v108, v84\n"
      "v_exp_f32 v109, v85\n"
      "s_waitcnt lgkmcnt(1)\n"
      "v_mfma_f32_32x32x16_bf16 v[64:79], %[k1], %[q1a], 0\n"
      "v_exp_f32 v110, v86\n"
      "v_exp_f32 v111, v87\n"
      "v_cvt_pk_bf16_f32 v98, v108, v109\n"
      "v_cvt_pk_bf16_f32 v99, v110, v111\n"
      "v_exp_f32 v104, v88\n"
      "v_exp_f32 v105, v89\n"
      "s_waitcnt lgkmcnt(0)\n"
      "v_mfma_f32_32x32x8_bf16 v[64:79], %[k2], %[q2a], v[64:79]\n"
      "v_exp_f32 v106, v90\n"
      "v_exp_f32 v107, v91\n"
      "v_cvt_pk_bf16_f32 v100, v104, v105\n"
      "v_cvt_pk_bf16_f32 v101, v106, v107\n"
      "v_exp_f32 v108, v92\n"
      "v_exp_f32 v109, v93\n"
      "v_mfma_f32_32x32x16_bf16 %[ob], %[v0], v[96:99], %[ob]\n"
      "ds_read_b128 %[v0], %[a16] offset:8704\n"
      "v_exp_f32 v110, v94\n"
      "v_exp_f32 v111, v95\n"
      "v_cvt_pk_bf16_f32 v102, v108, v109\n"
      "v_cvt_pk_bf16_f32 v103, v110, v111\n"
      "v_exp_f32 v104, v64\n"
      "v_exp_f32 v105, v65\n"
      "v_mfma_f32_32x32x16_bf16 %[ob], %[v1], v[100:103], %[ob]\n"
      "ds_read_b128 %[v1], %[a16] offset:9728\n"
      "v_exp_f32 v106, v66\n"
      "v_exp_f32 v107, v67\n"
      "v_cvt_pk_bf16_f32 v96, v104, v105\n"
      "v_cvt_pk_bf16_f32 v97, v106, v107\n"
      "v_exp_f32 v108, v68\n"
      "v_exp_f32 v109, v69\n"
      "v_mfma_f32_32x32x16_bf16 v[80:95], %[k1], %[q1b], 0\n"
      "v_exp_f32 v110, v70\n"
      "v_exp_f32 v111, v71\n"
      "v_cvt_pk_bf16_f32 v98, v108, v109\n"
      "v_cvt_pk_bf16_f32 v99, v110, v111\n"
      "v_exp_f32 v104, v72\n"
      "v_exp_f32 v105, v73\n"
      "v_mfma_f32_32x32x8_bf16 v[80:95], %[k2], %[q2b], v[80:95]\n"
      "ds_read_b128 %[k1], %[a16] offset:10752\n"
      "ds_read_b64 %[k2], %[a8] offset:11776\n"
      "v_exp_f32 v106, v74\n"
      "v_exp_f32 v107, v75\n"
      "v_cvt_pk_bf16_f32 v100, v104, v105\n"
      "v_cvt_pk_bf16_f32 v101, v106, v107\n"
      "v_exp_f32 v108, v76\n"
      "v_exp_f32 v109, v77\n"
      "s_waitcnt lgkmcnt(3)\n"
      "v_mfma_f32_32x32x16_bf16 %[oa], %[v0], v[96:99], %[oa]\n"
      "v_exp_f32 v110, v78\n"
      "v_exp_f32 v111, v79\n"
      "v_cvt_pk_bf16_f32 v102, v108, v109\n"
      "v_cvt_pk_bf16_f32 v103, v110, v111\n"
      "v_exp_f32 v104, v80\n"
      "v_exp_f32 v105, v81\n"
      "s_waitcnt lgkmcnt(2)\n"
      "v_mfma_f32_32x32x16_bf16 %[oa], %[v1], v[100:103], %[oa]\n"
      "v_exp_f32 v106, v82\n"
      "v_exp_f32 v107, v83\n"
      "v_cvt_pk_bf16_f32 v96, v104, v105\n"
      "v_cvt_pk_bf16_f32 v97, v106, v107\n"
      "v_exp_f32 v108, v84\n"
      "v_exp_f32 v109, v85\n"
      "s_waitcnt lgkmcnt(1)\n"
      "v_mfma_f32_32x32x16_bf16 v[64:79], %[k1], %[q1a], 0\n"
      "v_exp_f32 v110, v86\n"
      "v_exp_f32 v111, v87\n"
      "v_cvt_pk_bf16_f32 v98, v108, v109\n"
      "v_cvt_pk_bf16_f32 v99, v110, v111\n"
      "v_exp_f32 v104, v88\n"
      "v_exp_f32 v105, v89\n"
      "s_waitcnt lgkmcnt(0)\n"
      "v_mfma_f32_32x32x8_bf16 v[64:79], %[k2], %[q2a], v[64:79]\n"
      "v_exp_f32 v106, v90\n"
      "v_exp_f32 v107, v91\n"
      "v_cvt_pk_bf16_f32 v100, v104, v105\n"
      "v_cvt_pk_bf16_f32 v101, v106, v107\n"
      "v_exp_f32 v108, v92\n"
      "v_exp_f32 v109, v93\n"
      "v_mfma_f32_32x32x16_bf16 %[ob], %[v0], v[96:99], %[ob]\n"
      "ds_read_b128 %[v0], %[a16] offset:12288\n"
      "v_exp_f32 v110, v94\n"
      "v_exp_f32 v111, v95\n"
      "v_cvt_pk_bf16_f32 v102, v108, v109\n"
      "v_cvt_pk_bf16_f32 v103, v110, v111\n"
      "s_cmp_eq_u32 %[st], 7\n"
      "s_cbranch_scc1 2f\n"
      "s_add_u32 %[tmp], %[st], 2\n"
      "3:\n"
      "ds_read_b32 %[fv], %[fa]\n"
      "s_waitcnt lgkmcnt(0)\n"
      "v_readfirstlane_b32 %[t2], %[fv]\n"
      "s_cmp_ge_u32 %[t2], %[tmp]\n"
      "s_cbranch_scc1 2f\n"
      "s_sleep 1\n"
      "s_branch 3b\n"
      "2:\n"
      "v_exp_f32 v104, v64\n"
      "v_exp_f32 v105, v65\n"
      "v_mfma_f32_32x32x16_bf16 %[ob], %[v1], v[100:103], %[ob]\n"
      "ds_read_b128 %[v1], %[a16] offset:13312\n"
      "v_exp_f32 v106, v66\n"
      "v_exp_f32 v107, v67\n"
      "v_cvt_pk_bf16_f32 v96, v104, v105\n"
      "v_cvt_pk_bf16_f32 v97, v106, v107\n"
      "v_exp_f32 v108, v68\n"
      "v_exp_f32 v109, v69\n"
      "v_mfma_f32_32x32x16_bf16 v[80:95], %[k1], %[q1b], 0\n"
      "v_exp_f32 v110, v70\n"
      "v_exp_f32 v111, v71\n"
      "v_cvt_pk_bf16_f32 v98, v108, v109\n"
      "v_cvt_pk_bf16_f32 v99, v110, v111\n"
      "v_exp_f32 v104, v72\n"
      "v_exp_f32 v105, v73\n"
      "v_mfma_f32_32x32x8_bf16 v[80:95], %[k2], %[q2b], v[80:95]\n"
      "ds_read_b128 %[k1], %[n16]\n"
      "ds_read_b64 %[k2], %[n8] offset:1024\n"
      "v_exp_f32 v106, v74\n"
      "v_exp_f32 v107, v75\n"
      "v_cvt_pk_bf16_f32 v100, v104, v105\n"
      "v_cvt_pk_bf16_f32 v101, v106, v107\n"
      "v_exp_f32 v108, v76\n"
      "v_exp_f32 v109, v77\n"
      "s_waitcnt lgkmcnt(3)\n"
      "v_mfma_f32_32x32x16_bf16 %[oa], %[v0], v[96:99], %[oa]\n"
      "v_exp_f32 v110, v78\n"
      "v_exp_f32 v111, v79\n"
      "v_cvt_pk_bf16_f32 v102, v108, v109\n"
      "v_cvt_pk_bf16_f32 v103, v110, v111\n"
      "v_exp_f32 v104, v80\n"
      "v_exp_f32 v105, v81\n"
      "s_waitcnt lgkmcnt(2)\n"
      "v_mfma_f32_32x32x16_bf16 %[oa], %[v1], v[100:103], %[oa]\n"
      "v_exp_f32 v106, v82\n"
      "v_exp_f32 v107, v83\n"
      "v_cvt_pk_bf16_f32 v96, v104, v105\n"
      "v_cvt_pk_bf16_f32 v97, v106, v107\n"
      "v_exp_f32 v108, v84\n"
      "v_exp_f32 v109, v85\n"
      "s_waitcnt lgkmcnt(1)\n"
      "v_mfma_f32_32x32x16_bf16 v[64:79], %[k1], %[q1a], 0\n"
      "v_exp_f32 v110, v86\n"
      "v_exp_f32 v111, v87\n"
      "v_cvt_pk_bf16_f32 v98, v108, v109\n"
      "v_cvt_pk_bf16_f32 v99, v110, v111\n"
      "v_exp_f32 v104, v88\n"
      "v_exp_f32 v105, v89\n"
      "s_waitcnt lgkmcnt(0)\n"
      "v_mfma_f32_32x32x8_bf16 v[64:79], %[k2], %[q2a], v[64:79]\n"
      "v_exp_f32 v106, v90\n"
      "v_exp_f32 v107, v91\n"
      "v_cvt_pk_bf16_f32 v100, v104, v105\n"
      "v_cvt_pk_bf16_f32 v101, v106, v107\n"
      "v_exp_f32 v108, v92\n"
      "v_exp_f32 v109, v93\n"
      "v_mfma_f32_32x32x16_bf16 %[ob], %[v0], v[96:99], %[ob]\n"
      "ds_read_b128 %[v0], %[n16] offset:1536\n"
      "v_exp_f32 v110, v94\n"
      "v_exp_f32 v111, v95\n"
      "v_cvt_pk_bf16_f32 v102, v108, v109\n"
      "v_cvt_pk_bf16_f32 v103, v110, v111\n"
      "s_add_u32 %[st], %[st], 1\n"
      "v_mov_b32 %[fv], %[st]\n"
      "ds_write_b32 %[da], %[fv]\n"
      "s_mov_b32 %[tmp], %[b0]\n"
      "s_mov_b32 %[b0], %[b1]\n"
      "s_mov_b32 %[b1], %[b2]\n"
      "s_mov_b32 %[b2], %[tmp]\n"
      "s_cmp_lt_u32 %[st], 8\n"
      "s_cbranch_scc1 1b\n"
      "s_nop 1\n"
      "v_mfma_f32_32x32x16_bf16 %[ob], %[v1], v[100:103], %[ob]\n"
      "s_waitcnt lgkmcnt(0)\n"
      "s_nop 15\n"
      : [oa] "+v"(oa), [ob] "+v"(ob), [k1] "=&v"(k1), [k2] "=&v"(k2), [v0] "=&v"(v0), [v1] "+v"(v1),
        [a16] "=&v"(a16), [a8] "=&v"(a8), [n16] "=&v"(n16), [n8] "=&v"(n8),
        [b0] "+s"(b0), [b1] "+s"(b1), [b2] "+s"(b2), [st] "=&s"(st), [tmp] "=&s"(tmp), [t2] "=&s"(t2), [fv] "=&v"(fv)
      : [q1a] "v"(q1a), [q2a] "v"(q2a), [q1b] "v"(q1b), [q2b] "v"(q2b), [base16] "v"(base16), [base8] "v"(base8),
        [fa] "v"(fa), [da] "v"(da)
      : "memory", "scc", "v64", "v65", "v66", "v67", "v68", "v69", "v70", "v71", "v72", "v73", "v74", "v75", "v76", "v77", "v78", "v79", "v80", "v81", "v82", "v83", "v84", "v85", "v86", "v87", "v88", "v89", "v90", "v91", "v92", "v93", "v94", "v95", "v96", "v97", "v98", "v99", "v100", "v101", "v102", "v103", "v104", "v105", "v106", "v107", "v108", "v109", "v110", "v111");
  }
  float* ex = (float*)(lds + K2_EX + pr * 6144);
  if (kh == 0) {
#pragma unroll
    for (int r4 = 0; r4 < 3; ++r4) {
      *(f32x4*)(ex + r4 * 256 + lane * 4) = f32x4{oa[4 * r4], oa[4 * r4 + 1], oa[4 * r4 + 2], oa[4 * r4 + 3]};
      *(f32x4*)(ex + 768 + r4 * 256 + lane * 4) = f32x4{ob[4 * r4], ob[4 * r4 + 1], ob[4 * r4 + 2], ob[4 * r4 + 3]};
    }
    asm volatile("s_waitcnt lgkmcnt(0)" ::: "memory");
    fl[2 + pr] = 1u;
    return;
  }
  while (fl[2 + pr] == 0u) __builtin_amdgcn_s_sleep(1);
  asm volatile("" ::: "memory");
  const int h = lane & 31, hf = lane >> 5;
  const int s0 = sb + pr * 2;
  char* img = lds + K2_QI + pr * 2560;
#pragma unroll
  for (int a = 0; a < 2; ++a) {
    const f32x16& mine = a ? ob : oa;
    float o[12];
#pragma unroll
    for (int r4 = 0; r4 < 3; ++r4) {
      const f32x4 v = *(const f32x4*)(ex + a * 768 + r4 * 256 + lane * 4);
#pragma unroll
      for (int r = 0; r < 4; ++r) o[4 * r4 + r] = mine[4 * r4 + r] + v[r];
    }
    const int s = s0 + a;
    const float l = x32_sum(hf ? o[8] : 0.f);
    char* orow = img + a * 1280 + h * 40;
    const bool bad = !(l >= 8.6736174e-19f) || !(l <= 3.0e38f);
    if (__builtin_expect(__any(bad), 0)) {
      const char* qb = ws + WS_QP + (size_t)(s >> 5) * 80 * 512 + (s & 31) * 16;
      float q[20], oo[20];
#pragma unroll
      for (int d = 0; d < 20; ++d) { const int col = 20 * h + d; q[d] = bf2f(*(const __bf16*)(qb + (col >> 3) * 512 + (col & 7) * 2)); oo[d] = 0.f; }
      float m = -3.0e38f, ls = 0.f;
#pragma unroll 1
      for (int kk = 0; kk < S_; ++kk) {
        const char* tb = KVf + (size_t)(kk >> 5) * KV_TILE;
        const int kl = kk & 31;
        float sc = 0.f;
#pragma unroll
        for (int d = 0; d < 16; ++d) sc += q[d] * bf2f(*(const __bf16*)(tb + (kl + 32 * (d >> 3)) * 16 + (d & 7) * 2));
#pragma unroll
        for (int d = 16; d < 20; ++d) sc += q[d] * bf2f(*(const __bf16*)(tb + 1024 + kl * 8 + (d - 16) * 2));
        const float mn = fmaxf(m, sc), al = __builtin_amdgcn_exp2f(m - mn), p = __builtin_amdgcn_exp2f(sc - mn);
        const int sx = kl >> 4, k16 = kl & 15, vh = (k16 >> 2) & 1, jj = (k16 & 3) + 4 * (k16 >> 3);
        ls = ls * al + p;
#pragma unroll
        for (int d = 0; d < 20; ++d) oo[d] = oo[d] * al + p * bf2f(*(const __bf16*)(tb + 1536 + sx * 1024 + (d + 32 * vh) * 16 + jj * 2));
        m = mn;
      }
      const float inv = 1.f / ls;
      if (hf == 0) {
#pragma unroll
        for (int d4 = 0; d4 < 5; ++d4) *(bf16x4*)(orow + d4 * 8) = pack4(oo[4 * d4] * inv, oo[4 * d4 + 1] * inv, oo[4 * d4 + 2] * inv, oo[4 * d4 + 3] * inv);
      }
    } else {
      const float inv = __builtin_amdgcn_rcpf(l);
      *(bf16x4*)(orow + hf * 8) = pack4(o[0] * inv, o[1] * inv, o[2] * inv, o[3] * inv);
      *(bf16x4*)(orow + 16 + hf * 8) = pack4(o[4] * inv, o[5] * inv, o[6] * inv, o[7] * inv);
      if (hf == 0) *(bf16x4*)(orow + 32) = pack4(o[8] * inv, o[9] * inv, o[10] * inv, o[11] * inv);
    }
  }
  asm volatile("s_waitcnt lgkmcnt(0)" ::: "memory");
  {
    char* op = ws + WS_OB + (size_t)s0 * 1280 + lane * 16;
    st16(op, *(const u32x4*)(img + lane * 16));
    st16(op + 1024, *(const u32x4*)(img + 1024 + lane * 16));
    if (lane < 32) st16(op + 2048, *(const u32x4*)(img + 2048 + lane * 16));
  }
}

constexpr int K3_XB = 24576;

__global__ __launch_bounds__(512, 2) void k3_out(const __bf16* __restrict__ woA, const __bf16* __restrict__ woB,
                                                 char* __restrict__ ws, __bf16* __restrict__ out) {
  __shared__ __attribute__((aligned(16))) char lds[G_NB * G_STAGE];
  const int tid = threadIdx.x, lane = tid & 63, w = __builtin_amdgcn_readfirstlane(tid >> 6);
  const int T = (blockIdx.x & 7) * 28 + (blockIdx.x >> 3);
  if (T >= 220) return;
  const int rt = (T * 205) >> 11, ct = T - rt * 10;
  const unsigned lbase = (unsigned)(size_t)lds;
  const int l15 = lane & 15, g = lane >> 4;
  if (__builtin_expect(w >= 4, 1)) {
    const int lw = w - 4;
    const unsigned wv0 = (lane >> 3) * 1280 + ((((lane & 7) >> 1) ^ ((lane >> 4) & 1)) << 5) + ((lane & 1) << 4), wv1 = wv0 ^ 64;
    unsigned aoff[6], al[6];
#pragma unroll
    for (int ii = 0; ii < 6; ++ii) {
      int p = lw * 6 + ii + ct; p = p >= 24 ? p - 24 : p;
      const int row = 4 * p + (lane >> 4);
      int s = rt * 96 + row; s = s > S_ - 1 ? S_ - 1 : s;
      aoff[ii] = s * 1280 + (((lane & 15) ^ (row & 15)) << 4);
      al[ii] = p * 1024;
    }
    auto issue = [&](int st, int buf) {
      const int part = st >= 5, kk = st - 5 * part;
      const char* ab = ws + (part ? WS_OB : WS_OA) + kk * 256;
      const char* wp = (const char*)(part ? woB : woA) + (size_t)(kk * 128 + lw * 32) * 1280 + ct * 128;
      const unsigned lb = lbase + buf * G_STAGE;
      const unsigned wl = lb + K3_XB + lw * 4096;
      dma16(ab, aoff[0], lb + al[0]); dma16(wp, wv0, wl); dma16(ab, aoff[1], lb + al[1]);
      dma16(ab, aoff[2], lb + al[2]); dma16(wp + 8 * 1280, wv1, wl + 1024); dma16(ab, aoff[3], lb + al[3]);
      dma16(wp + 16 * 1280, wv0, wl + 2048); dma16(ab, aoff[4], lb + al[4]); dma16(ab, aoff[5], lb + al[5]);
      dma16(wp + 24 * 1280, wv1, wl + 3072);
    };
    issue(0, 0); issue(1, 1);
    WAIT_BAR(10);
    issue(2, 2);
    WAIT_BAR(10);
#pragma unroll 1
    for (int st = 0; st < 9; ++st) {
      if (st < 7) { issue(st + 3, (st + 3) & 3); WAIT_BAR(10); }
      else { WAIT_BAR(0); }
    }
  } else {
    const int wm = w >> 1, wn = w & 1;
    const int p4 = lane & 3, q4 = l15 >> 2;
    const int xo0 = (wm * 48 + l15) * 256 + ((g ^ l15) << 4);
    const int f = ((q4 >> 1) & 1) | ((g & 1) << 1);
    int wb[2];
#pragma unroll
    for (int j = 0; j < 2; ++j) wb[j] = K3_XB + (8 * g + q4) * 128 + 8 * p4 + 32 * ((2 * wn + j) ^ f);
    float gv[3];
#pragma unroll
    for (int mi = 0; mi < 3; ++mi) {
      int s = rt * 96 + wm * 48 + mi * 16 + l15; s = s > S_ - 1 ? S_ - 1 : s;
      const float gg = ((const float*)(ws + WS_G))[s];
      gv[mi] = gg;
    }
    f32x4 acc[3][2] = {}, accA[3][2];
    bf16x8 xf[4][3], wf[4][2];
    auto ldf = [&](int set, const char* sb, int ks) {
#pragma unroll
      for (int mi = 0; mi < 3; ++mi) xf[set][mi] = *(const bf16x8*)(sb + (xo0 ^ (ks * 64)) + mi * 4096);
#pragma unroll
      for (int ni = 0; ni < 2; ++ni) {
        const char* pw = sb + wb[ni] + ks * 32 * 128;
        wf[set][ni] = cat8(trread(pw), trread(pw + 4 * 128));
      }
    };
    auto mm = [&](int set) {
#pragma unroll
      for (int mi = 0; mi < 3; ++mi)
#pragma unroll
        for (int ni = 0; ni < 2; ++ni) acc[mi][ni] = mfma16(wf[set][ni], xf[set][mi], acc[mi][ni]);
#pragma unroll
      for (int i = 0; i < 6; ++i) { __builtin_amdgcn_sched_group_barrier(0x8, 1, 0); __builtin_amdgcn_sched_group_barrier(0x100, 1, 0); }
      __builtin_amdgcn_sched_group_barrier(0x100, 1, 0);
      __builtin_amdgcn_sched_barrier(0);
    };
    WAIT_BAR(10);
    ldf(0, lds, 0); ldf(1, lds, 1);
#pragma unroll
    for (int st = 0; st < 10; ++st) {
      const char* sb = lds + (st & 3) * G_STAGE;
      const char* sn = lds + ((st + 1) & 3) * G_STAGE;
      if (st == 5) {
#pragma unroll
        for (int mi = 0; mi < 3; ++mi)
#pragma unroll
          for (int ni = 0; ni < 2; ++ni) { accA[mi][ni] = acc[mi][ni] * gv[mi]; acc[mi][ni] = f32x4{0.f, 0.f, 0.f, 0.f}; }
      }
      ldf(2, sb, 2); mm(0);
      ldf(3, sb, 3); mm(1);
      if (st == 0) RAWBAR();
      if (st < 9) ldf(0, sn, 0);
      mm(2);
      if (st < 9) ldf(1, sn, 1);
      mm(3);
      if (st < 9) RAWBAR();
    }
    const int s0 = rt * 96 + wm * 48 + l15;
    char* dst = (char*)out + (size_t)s0 * 1280 + ct * 128 + (wn * 32 + (g & 1) * 16 + (g >> 1) * 8) * 2;
#pragma unroll
    for (int mi = 0; mi < 3; ++mi) {
      const u32x4 v = pair_pack(accA[mi][0] + acc[mi][0] * (1.f - gv[mi]), accA[mi][1] + acc[mi][1] * (1.f - gv[mi]));
      if (s0 + mi * 16 < S_) st16(dst + (size_t)mi * 16 * 1280, v);
    }
  }
}

extern "C" void kernel_launch(void* const* d_in, const int* in_sizes, int n_in, void* d_out, int out_size, void* d_ws,
                              size_t ws_size, hipStream_t stream) {
  const __bf16* x = (const __bf16*)d_in[0];
  const __bf16* wqkv = (const __bf16*)d_in[1];
  const __bf16* woA = (const __bf16*)d_in[2];
  const __bf16* wq = (const __bf16*)d_in[4];
  const __bf16* wk = (const __bf16*)d_in[5];
  const __bf16* wv = (const __bf16*)d_in[6];
  const __bf16* woB = (const __bf16*)d_in[7];
  const __bf16* gw = (const __bf16*)d_in[8];
  char* ws = (char*)d_ws;
  k1_proj<<<256, 512, 0, stream>>>(x, wqkv, wq, wk, wv, gw, ws);
  k2_attn<<<256, 832, 0, stream>>>(ws, (const char*)woA, (const char*)woB);
  k3_out<<<224, 512, 0, stream>>>(woA, woB, ws, (__bf16*)d_out);
}
```

```cpp
#include <hip/hip_runtime.h>
#include <hip/hip_bf16.h>
#include <stdint.h>

#define DI __device__ __forceinline__
typedef __attribute__((ext_vector_type(8))) __bf16 bf16x8;
typedef __attribute__((ext_vector_type(4))) __bf16 bf16x4;
typedef __attribute__((ext_vector_type(4))) short s16x4;
typedef __attribute__((ext_vector_type(8))) short s16x8;
typedef __attribute__((ext_vector_type(16))) float f32x16;
typedef __attribute__((ext_vector_type(4))) float f32x4;
typedef __attribute__((ext_vector_type(4))) unsigned u32x4;
typedef __attribute__((ext_vector_type(2))) unsigned u32x2;
typedef __attribute__((ext_vector_type(16))) unsigned u32x16;
typedef __attribute__((address_space(3))) s16x4* lds_s16x4_ptr;

constexpr int S_ = 2048, D_ = 640;
constexpr float QSCALE = 0.22360679774997896f * 1.4426950408889634f;

constexpr size_t WS_QP = 0;
constexpr size_t WS_QKV = 2048 * 1280;
constexpr size_t WS_KV = WS_QKV + 2048 * 3840;
constexpr size_t WS_G = 13u << 20;
constexpr size_t WS_KMAX = (13u << 20) + 65536;
constexpr size_t WS_OA = 32u << 20;
constexpr size_t WS_OB = 36u << 20;
constexpr int KV_TILE = 3584;

DI f32x4 mfma16(bf16x8 a, bf16x8 b, f32x4 c) { return __builtin_amdgcn_mfma_f32_16x16x32_bf16(a, b, c, 0, 0, 0); }
DI f32x16 mfma32(bf16x8 a, bf16x8 b, f32x16 c) { return __builtin_amdgcn_mfma_f32_32x32x16_bf16(a, b, c, 0, 0, 0); }
DI s16x4 trread(const char* p) { return __builtin_amdgcn_ds_read_tr16_b64_v4i16((lds_s16x4_ptr)p); }
DI bf16x8 cat8(s16x4 lo, s16x4 hi) { return __builtin_bit_cast(bf16x8, __builtin_shufflevector(lo, hi, 0, 1, 2, 3, 4, 5, 6, 7)); }
DI bf16x4 pack4(float a, float b, float c, float d) { bf16x4 r; r[0] = (__bf16)a; r[1] = (__bf16)b; r[2] = (__bf16)c; r[3] = (__bf16)d; return r; }
DI float bf2f(__bf16 v) { return (float)v; }
DI float x16_max(float x) {
  auto s = __builtin_amdgcn_permlane16_swap(__float_as_uint(x), __float_as_uint(x), false, false);
  x = fmaxf(__uint_as_float(s[0]), __uint_as_float(s[1]));
  auto t = __builtin_amdgcn_permlane32_swap(__float_as_uint(x), __float_as_uint(x), false, false);
  return fmaxf(__uint_as_float(t[0]), __uint_as_float(t[1]));
}
DI float x16_sum(float x) {
  auto s = __builtin_amdgcn_permlane16_swap(__float_as_uint(x), __float_as_uint(x), false, false);
  x = __uint_as_float(s[0]) + __uint_as_float(s[1]);
  auto t = __builtin_amdgcn_permlane32_swap(__float_as_uint(x), __float_as_uint(x), false, false);
  return __uint_as_float(t[0]) + __uint_as_float(t[1]);
}
DI float x32_sum(float x) {
  auto t = __builtin_amdgcn_permlane32_swap(__float_as_uint(x), __float_as_uint(x), false, false);
  return __uint_as_float(t[0]) + __uint_as_float(t[1]);
}
template <int CTRL> DI float dppf(float x) { return __builtin_bit_cast(float, __builtin_amdgcn_mov_dpp(__builtin_bit_cast(int, x), CTRL, 0xf, 0xf, true)); }
DI float row16_max(float x) {
  x = fmaxf(x, dppf<0xB1>(x)); x = fmaxf(x, dppf<0x4E>(x)); x = fmaxf(x, dppf<0x141>(x)); x = fmaxf(x, dppf<0x140>(x));
  return x;
}

DI u32x4 pair_pack(const f32x4& a, const f32x4& b) {
  u32x2 pa = __builtin_bit_cast(u32x2, pack4(a[0], a[1], a[2], a[3])), pb = __builtin_bit_cast(u32x2, pack4(b[0], b[1], b[2], b[3]));
  auto s0 = __builtin_amdgcn_permlane16_swap(pa[0], pb[0], false, false);
  auto s1 = __builtin_amdgcn_permlane16_swap(pa[1], pb[1], false, false);
  return u32x4{s0[0], s1[0], s0[1], s1[1]};
}
DI u32x4 quad_pack(const f32x16& a, int q) {
  u32x2 pa = __builtin_bit_cast(u32x2, pack4(a[4 * q], a[4 * q + 1], a[4 * q + 2], a[4 * q + 3]));
  u32x2 pb = __builtin_bit_cast(u32x2, pack4(a[4 * q + 4], a[4 * q + 5], a[4 * q + 6], a[4 * q + 7]));
  auto s0 = __builtin_amdgcn_permlane32_swap(pa[0], pb[0], false, false);
  auto s1 = __builtin_amdgcn_permlane32_swap(pa[1], pb[1], false, false);
  return u32x4{s0[0], s1[0], s0[1], s1[1]};
}
DI void st16(void* p, u32x4 v) { asm volatile("global_store_dwordx4 %0, %1, off sc1\n\ts_nop 1" :: "v"(p), "v"(v) : "memory"); }
DI void st8(void* p, u32x2 v) { asm volatile("global_store_dwordx2 %0, %1, off sc1" :: "v"(p), "v"(v) : "memory"); }
DI const void* uni_ptr(const void* p) {
  const unsigned long long a = (unsigned long long)p;
  const unsigned lo = __builtin_amdgcn_readfirstlane((unsigned)a), hi = __builtin_amdgcn_readfirstlane((unsigned)(a >> 32));
  return (const void*)(((unsigned long long)hi << 32) | lo);
}
DI void dma16(const void* sbase, unsigned voff, unsigned lds_dst) {
  sbase = uni_ptr(sbase); lds_dst = __builtin_amdgcn_readfirstlane(lds_dst);
  unsigned keep;
  asm volatile("s_mov_b32 %0, m0\n\ts_mov_b32 m0, %3\n\ts_nop 2\n\tglobal_load_lds_dwordx4 %1, %2\n\ts_mov_b32 m0, %0"
               : "=&s"(keep) : "v"(voff), "s"(sbase), "s"(lds_dst) : "memory");
}
DI void dma4(const void* sbase, unsigned voff, unsigned lds_dst) {
  sbase = uni_ptr(sbase); lds_dst = __builtin_amdgcn_readfirstlane(lds_dst);
  unsigned keep;
  asm volatile("s_mov_b32 %0, m0\n\ts_mov_b32 m0, %3\n\ts_nop 2\n\tglobal_load_lds_dword %1, %2\n\ts_mov_b32 m0, %0"
               : "=&s"(keep) : "v"(voff), "s"(sbase), "s"(lds_dst) : "memory");
}
#define WAIT_BAR(n) asm volatile("s_waitcnt vmcnt(" #n ") lgkmcnt(0)\n\ts_barrier" ::: "memory")
#define BAR() asm volatile("s_waitcnt lgkmcnt(0)\n\ts_barrier" ::: "memory")
#define RAWBAR() asm volatile("s_barrier" ::: "memory")

constexpr int G_STAGE = 40960, G_NB = 4;
constexpr int K1_XB = 16384;

DI void k1_kv_block(char* lds, int kvb, int w, int lane, const __bf16* x, const __bf16* wk, const __bf16* wv,
                    const __bf16* gw, char* KVf, float* gout, float* kmax2) {
  const unsigned lbase = (unsigned)(size_t)lds;
  const int tid = w * 64 + lane, l15 = lane & 15, g = lane >> 4;
  constexpr int WK = 81920, WV = WK + 25600, GW = WV + 25600;
  {
    const char* xb = (const char*)x + (size_t)kvb * 64 * 1280;
#pragma unroll 2
    for (int p = w; p < 80; p += 12) {
      const int q = p * 64 + lane, row = q / 80, cp = q - row * 80;
      dma16(xb, row * 1280 + (((cp & ~7) | ((cp ^ row) & 7)) << 4), lbase + p * 1024);
    }
    for (int p = w; p < 50; p += 12) {
      const char* src = (p < 25) ? (const char*)wk + p * 1024 : (const char*)wv + (p - 25) * 1024;
      dma16(src, lane * 16, lbase + WK + p * 1024);
    }
    for (int k = tid; k < 640; k += 768) {
      bf16x4 v; v[0] = gw[k]; v[1] = (__bf16)0.f; v[2] = (__bf16)0.f; v[3] = (__bf16)0.f;
      *(bf16x4*)(lds + GW + k * 40) = v;
    }
  }
  WAIT_BAR(0);
  f32x4 acc[3] = {};
  const int row = (w & 3) * 16 + l15;
  if (w < 4) {
    const int p4 = lane & 3, q4 = l15 >> 2;
    int wa[3];
#pragma unroll
    for (int ni = 0; ni < 3; ++ni) {
      const int cg = 4 * ni + p4;
      wa[ni] = (cg < 5 ? WK + cg * 8 : (cg < 10 ? WV + (cg - 5) * 8 : GW)) + (8 * g + q4) * 40;
    }
    const int xo = row * 1280 + ((g ^ (row & 7)) << 4);
#pragma unroll
    for (int ks = 0; ks < 20; ++ks) {
      const bf16x8 xf = *(const bf16x8*)(lds + (xo ^ ((ks & 1) * 64)) + (ks >> 1) * 128);
#pragma unroll
      for (int ni = 0; ni < 3; ++ni) {
        const char* pw = lds + wa[ni] + ks * 32 * 40;
        acc[ni] = mfma16(cat8(trread(pw), trread(pw + 160)), xf, acc[ni]);
      }
    }
  }
  BAR();
  if (w < 4) {
#pragma unroll
    for (int ni = 0; ni < 3; ++ni) {
      const int cg = 4 * ni + g;
      const bf16x4 v = pack4(acc[ni][0], acc[ni][1], acc[ni][2], acc[ni][3]);
      if (cg < 5) *(bf16x4*)(lds + row * 96 + cg * 8) = v;
      else if (cg < 10) *(bf16x4*)(lds + row * 96 + 48 + (cg - 5) * 8) = v;
      else if (cg == 10) gout[kvb * 64 + row] = 1.f / (1.f + __expf(-acc[ni][0]));
    }
  }
  BAR();
  if (w >= 2) return;
  const char* T = lds + w * 32 * 96;
  const int t = kvb * 2 + w;
  char* dst = KVf + (size_t)t * KV_TILE;
  const int key = lane & 31, hf = lane >> 5;
  const u32x4 k1 = *(const u32x4*)(T + key * 96 + hf * 16);
  u32x2 k2 = *(const u32x2*)(T + key * 96 + 32);
  {
    const bf16x8 a = __builtin_bit_cast(bf16x8, k1); const bf16x4 b = __builtin_bit_cast(bf16x4, k2);
    float n = 0.f;
#pragma unroll
    for (int j = 0; j < 8; ++j) n += bf2f(a[j]) * bf2f(a[j]);
    float n3 = 0.f;
#pragma unroll
    for (int j = 0; j < 4; ++j) n3 += bf2f(b[j]) * bf2f(b[j]);
    n = x32_sum(n) + n3;
    n = x16_max(row16_max(n));
    if (lane == 0) kmax2[t] = n;
  }
  st16(dst + lane * 16, k1);
  if (hf) { k2[0] = 0x3F80u; k2[1] = 0u; }
  st8(dst + 1024 + lane * 8, k2);
#pragma unroll
  for (int s = 0; s < 2; ++s) {
    s16x8 v;
#pragma unroll
    for (int j = 0; j < 8; ++j) {
      const int kk = 16 * s + 8 * (j >> 2) + 4 * hf + (j & 3);
      const short e = *(const short*)(T + kk * 96 + 48 + (key < 20 ? key : 0) * 2);
      v[j] = key < 20 ? e : (key == 20 ? (short)0x3F80 : (short)0);
    }
    st16(dst + 1536 + s * 1024 + lane * 16, __builtin_bit_cast(u32x4, v));
  }
}

__global__ __launch_bounds__(768, 3) void k1_proj(const __bf16* __restrict__ x, const __bf16* __restrict__ wqkv,
                                                  const __bf16* __restrict__ wq, const __bf16* __restrict__ wk,
                                                  const __bf16* __restrict__ wv, const __bf16* __restrict__ gw,
                                                  char* __restrict__ ws) {
  __shared__ __attribute__((aligned(16))) char lds[G_NB * G_STAGE];
  const int tid = threadIdx.x, lane = tid & 63, w = __builtin_amdgcn_readfirstlane(tid >> 6);
  const int b = blockIdx.x, xcd = b & 7, idx = b >> 3;
  if (__builtin_expect(idx >= 28, 0)) {
    k1_kv_block(lds, xcd * 4 + (idx - 28), w, lane, x, wk, wv, gw, ws + WS_KV, (float*)(ws + WS_G), (float*)(ws + WS_KMAX));
    return;
  }
  const int rl = (idx * 37) >> 8, cl7 = idx - rl * 7;
  const int rt = 4 * (xcd >> 1) + rl, ct = 7 * (xcd & 1) + cl7;
  const unsigned lbase = (unsigned)(size_t)lds;
  const int l15 = lane & 15, g = lane >> 4;
  if (__builtin_expect(w >= 8, 1)) {
    const int lw = w - 8;
    const int rX = cl7, rW = (0x5320 >> (4 * rl)) & 15;
    const char* xb0 = (const char*)x + (size_t)rt * 128 * 1280;
    const unsigned xe = (lane >> 3) * 1280 + (((lane & 7) ^ (lane >> 4)) << 4);
    const unsigned xv0 = xe ^ ((rX & 1) << 6), xv1 = xv0 ^ 64;
    const bool isq = ct >= 10;
    const unsigned wstride = isq ? 1280u : 3840u;
    const char* wb0 = (isq ? (const char*)wq + (ct - 10) * 384 : (const char*)wqkv + ct * 384);
    const unsigned wv0 = (lane >> 3) * wstride + ((((lane & 7) >> 1) ^ ((lane >> 4) & 1)) << 5) + ((lane & 1) << 4);
    auto issue = [&](int st, int buf) {
      const char* xb = xb0 + st * 128;
      const char* wp = wb0 + (size_t)st * 64 * wstride;
      const unsigned lb = lbase + buf * G_STAGE;
      auto xp = [&](int ii) { const int p = (lw * 4 + ii + rX) & 15; dma16(xb + p * 8 * 1280, (ii & 1) ? xv1 : xv0, lb + p * 1024); };
      auto wpc = [&](int ii) {
        int p = lw * 6 + ii + rW; p = p >= 24 ? p - 24 : p;
        const int ko = (p * 43) >> 7, th = p - ko * 3;
        dma16(wp + (size_t)ko * 8 * wstride + (ct == 13 ? 0 : th * 128), wv0 ^ ((ko & 1) << 6), lb + K1_XB + p * 1024);
      };
      xp(0); wpc(0); wpc(1); xp(1); wpc(2); xp(2); wpc(3); wpc(4); xp(3); wpc(5);
    };
    issue(0, 0); issue(1, 1);
    WAIT_BAR(10);
    issue(2, 2);
    WAIT_BAR(10);
#pragma unroll 1
    for (int st = 0; st < 9; ++st) {
      if (st < 7) { issue(st + 3, (st + 3) & 3); WAIT_BAR(10); }
      else { WAIT_BAR(0); }
    }
  } else {
    const int wm = w >> 2, wn = w & 3;
    const int p4 = lane & 3, q4 = l15 >> 2;
    const int xo0 = (wm * 64 + l15) * 128 + ((g ^ ((l15 >> 1) & 7)) << 4);
    int wo[3];
    {
      const int f = ((q4 >> 1) & 1) | ((g & 1) << 1);
#pragma unroll
      for (int ni = 0; ni < 3; ++ni) { const int bb = 3 * wn + ni; wo[ni] = K1_XB + (g * 3 + (bb >> 2)) * 1024 + q4 * 128 + (((bb & 3) ^ f) << 5) + 8 * p4; }
    }
    f32x4 acc[4][3] = {};
    bf16x8 xa[4], wa[3], xb[4], wc[3];
    auto ldf = [&](bf16x8 (&xf)[4], bf16x8 (&wf)[3], const char* sb, int ks) {
#pragma unroll
      for (int mi = 0; mi < 4; ++mi) xf[mi] = *(const bf16x8*)(sb + (xo0 ^ (ks * 64)) + mi * 2048);
#pragma unroll
      for (int ni = 0; ni < 3; ++ni) {
        const char* pw = sb + wo[ni] + ks * 12 * 1024;
        wf[ni] = cat8(trread(pw), trread(pw + 512));
      }
    };
    auto mm = [&](const bf16x8 (&xf)[4], const bf16x8 (&wf)[3]) {
#pragma unroll
      for (int mi = 0; mi < 4; ++mi)
#pragma unroll
        for (int ni = 0; ni < 3; ++ni) acc[mi][ni] = mfma16(wf[ni], xf[mi], acc[mi][ni]);
#pragma unroll
      for (int i = 0; i < 10; ++i) { __builtin_amdgcn_sched_group_barrier(0x8, 1, 0); __builtin_amdgcn_sched_group_barrier(0x100, 1, 0); }
      __builtin_amdgcn_sched_group_barrier(0x8, 2, 0);
      __builtin_amdgcn_sched_barrier(0);
    };
    RAWBAR();
    ldf(xa, wa, lds, 0);
#pragma unroll
    for (int st = 0; st < 10; ++st) {
      const char* sb = lds + (st & 3) * G_STAGE;
      const char* sn = lds + ((st + 1) & 3) * G_STAGE;
      ldf(xb, wc, sb, 1); mm(xa, wa);
      if (st == 0) RAWBAR();
      if (st < 9) ldf(xa, wa, sn, 0);
      mm(xb, wc);
      if (st < 9) RAWBAR();
    }
    char* dst; int no, oc0;
    if (ct >= 10) {
      dst = ws + WS_QP; no = 80; oc0 = (ct - 10) * 24;
#pragma unroll
      for (int mi = 0; mi < 4; ++mi)
#pragma unroll
        for (int ni = 0; ni < 3; ++ni) acc[mi][ni] *= QSCALE;
    } else { dst = ws + WS_QKV; no = 240; oc0 = ct * 24; }
    oc0 += wn * 6;
    dst += (size_t)((rt * 4 + wm * 2) * no) * 512 + l15 * 16;
    const int ocp = oc0 + (g & 1) * 2 + (g >> 1), ocs = oc0 + 4 + (g >> 1);
#pragma unroll
    for (int mi = 0; mi < 4; ++mi) {
      char* d = dst + (size_t)(mi >> 1) * no * 512 + (mi & 1) * 256;
      const u32x4 v = pair_pack(acc[mi][0], acc[mi][1]);
      if (ocp < no) st16(d + (size_t)ocp * 512, v);
      if (ocs < no) st8(d + (size_t)ocs * 512 + (g & 1) * 8, __builtin_bit_cast(u32x2, pack4(acc[mi][2][0], acc[mi][2][1], acc[mi][2][2], acc[mi][2][3])));
    }
  }
}

constexpr int K2_STAGE = 8 * KV_TILE;
constexpr unsigned A_START = 0;
constexpr int K2_RA = 3 * K2_STAGE;
constexpr int K2_QI = K2_RA + 38 * 1024;
constexpr int K2_EX = K2_QI + 10240;
constexpr int K2_FL = K2_EX + 8 * 3072;
constexpr int K2_SCR = K2_FL + 2304;

DI float dot2bf(unsigned a, float c) {
  typedef __attribute__((ext_vector_type(2))) __bf16 bf16x2;
  return __builtin_amdgcn_fdot2_f32_bf16(__builtin_bit_cast(bf16x2, a), __builtin_bit_cast(bf16x2, a), c, false);
}
DI void prep_q(const char* qi, int lane, float km, bf16x8& q1, s16x4& q2) {
  const int h = lane & 31, hf = lane >> 5;
  auto qa = [&](int col) { return *(const u32x2*)(qi + (col >> 3) * 128 + (col & 7) * 2); };
  const u32x2 lo = qa(20 * h + 8 * hf), hi = qa(20 * h + 8 * hf + 4), tl = qa(20 * h + 16);
  q1 = __builtin_bit_cast(bf16x8, u32x4{lo[0], lo[1], hi[0], hi[1]});
  const float n2 = x32_sum(dot2bf(lo[0], dot2bf(lo[1], dot2bf(hi[0], dot2bf(hi[1], 0.f))))) + dot2bf(tl[0], dot2bf(tl[1], 0.f));
  const float off = __builtin_amdgcn_sqrtf(n2 * km) * 1.01f - 60.f;
  unsigned ob = __float_as_uint(off);
  ob = (off > 0.f) ? ((ob + 0xFFFFu) & 0xFFFF0000u) : (ob & 0xFFFF0000u);
  u32x2 t2 = tl;
  if (hf) { t2[0] = (ob >> 16) ^ 0x8000u; t2[1] = 0u; }
  q2 = __builtin_bit_cast(s16x4, t2);
}

__global__ __launch_bounds__(832, 4) void k2_attn(char* __restrict__ ws, const char* __restrict__ woA, const char* __restrict__ woB) {
  __shared__ __attribute__((aligned(16))) char lds[K2_SCR + 256];
  const int tid = threadIdx.x, lane = tid & 63, w = __builtin_amdgcn_readfirstlane(tid >> 6);
  const unsigned lbase = (unsigned)(size_t)lds;
  const char* KVf = ws + WS_KV;
  volatile __attribute__((address_space(3))) unsigned* fl = (volatile __attribute__((address_space(3))) unsigned*)(lds + K2_FL);
  const int pr = w & 3, kh = (w >> 2) & 1;
  const int sb = blockIdx.x * 8;
  const int hp = blockIdx.x & 15, art = blockIdx.x >> 4;
  if (w < 8) {
    const char* qp = ws + WS_QP + (size_t)(sb >> 5) * 80 * 512 + (sb & 31) * 16;
#pragma unroll
    for (int ii = 0; ii < 2; ++ii) {
      int p = w + 8 * ii; p = p > 9 ? 9 : p;
      dma16(qp, (8 * p + (lane >> 3)) * 512 + (lane & 7) * 16, lbase + K2_QI + p * 1024);
    }
    const int wq = w > 6 ? 6 : w;
#pragma unroll
    for (int k = 0; k < 4; ++k) dma16(KVf + wq * 4096 + k * 1024, lane * 16, lbase + wq * 4096 + k * 1024);
  } else {
    for (int i = tid - 512; i < 576; i += 320) fl[i] = i == 0 ? 1u : 0u;
  }
  float km = ((const float*)(ws + WS_KMAX))[lane];
  km = x16_max(row16_max(km));
  asm volatile("" : "+v"(km));
  WAIT_BAR(0);
  if (w == 8) {
    __builtin_amdgcn_s_setprio(3);
    const unsigned dv = lane * 16;
#pragma unroll 1
    for (int st = 1; st < 8; ++st) {
      if (st >= 3) {
        while (true) {
          const unsigned d = fl[64 + 64 * (lane & 7)];
          if (__all(d >= (unsigned)(st - 2))) break;
          __builtin_amdgcn_s_sleep(2);
        }
      }
      const char* src = KVf + st * K2_STAGE;
      const unsigned ld = lbase + (st % 3) * K2_STAGE;
#pragma unroll 4
      for (int k = 0; k < 28; ++k) dma16(src + k * 1024, dv, ld + k * 1024);
      asm volatile("s_waitcnt vmcnt(0)" ::: "memory");
      fl[0] = st + 1;
      if (st == 4) {
        const int grp = blockIdx.x >> 3;
#pragma unroll
        for (int k = 0; k < 7; ++k) {
          int ln = k * 64 + lane; ln = ln > 399 ? 399 : ln;
          dma4(grp >= 16 ? woB : woA, ((grp & 15) * 400 + ln) * 128, lbase + K2_SCR);
        }
      }
    }
    return;
  }
  if (w > 8) {
    {
      const char* rap = ws + WS_QKV + ((long)art * 4 - 1) * 240 * 512 + hp * 5 * 512;
      unsigned rp[3];
#pragma unroll
      for (int t = 0; t < 3; ++t) { const int r = 16 + 64 * t + lane; rp[t] = (r >> 5) * 240 * 512 + (r & 31) * 16; }
#pragma unroll 1
      for (int co = w - 9; co < 15; co += 4) {
        const int seg = (co * 13) >> 6;
        const char* src = rap + (seg * 75 + co) * 512;
        const unsigned ld = lbase + K2_RA + co * 2560;
        dma16(src, rp[0], ld); dma16(src, rp[1], ld + 1024);
        if (lane < 32) dma16(src, rp[2], ld + 2048);
      }
      asm volatile("s_waitcnt vmcnt(0)" ::: "memory");
      if (lane == 0) __hip_atomic_fetch_add((__attribute__((address_space(3))) unsigned*)(lds + K2_FL + 4), 1u, __ATOMIC_RELAXED, __HIP_MEMORY_SCOPE_WORKGROUP);
    }
    while (fl[64 + 64 * (w & 3)] < A_START) __builtin_amdgcn_s_sleep(16);
    while (fl[1] < 4u) __builtin_amdgcn_s_sleep(8);
    const int l15 = lane & 15, g = lane >> 4, p4 = lane & 3, q4 = l15 >> 2;
    const int hh = w & 1, par = ((w - 9) >> 1) & 1;
    constexpr int TS = 4 * 128;
    const char* RA = lds + K2_RA;
    auto ea = [&](int u, int cc) { const int i = 2 * u + par; return RA + ((cc >> 3) * 20 + (i >> 3)) * 128 + (i & 7) * 16 + (cc & 7) * 2; };
    const int gk = g < 3 ? 8 * g : 0;
    const char* klo = ea(l15, 40 + 20 * hh + gk);
    const char* khi = ea(l15, 40 + 20 * hh + gk + 4);
    const char* vp0 = ea(4 * g + q4, 80 + 20 * hh + 4 * p4);
    const char* vp1 = ea(4 * g + q4, 80 + 20 * hh + 16 + (hh == 1 && p4 > 0 ? 0 : 4 * p4));
    const char* qlo = ea(8 + l15, 20 * hh + gk);
    const char* qhi = ea(8 + l15, 20 * hh + gk + 4);
    bool m0[4], m1[4];
#pragma unroll
    for (int r = 0; r < 4; ++r) { const int j = 4 * g + r; m0[r] = (j >= l15 + 1) && (j <= l15 + 8); m1[r] = (j <= l15 - 8); }
          const f32x4 z4 = {0.f, 0.f, 0.f, 0.f};
    const s16x4 zz = {0, 0, 0, 0};
    const float ninf = -__builtin_inff();
    bf16x8 kc = cat8(*(const s16x4*)klo, *(const s16x4*)khi);
    s16x4 vc0 = trread(vp0), vc1 = trread(vp1);
#pragma unroll
    for (int y = 0; y < 4; ++y) {
      const bf16x8 kn = cat8(*(const s16x4*)(klo + (y + 1) * TS), *(const s16x4*)(khi + (y + 1) * TS));
      const s16x4 vn0 = trread(vp0 + (y + 1) * TS), vn1 = trread(vp1 + (y + 1) * TS);
      s16x4 a = *(const s16x4*)(qlo + y * TS), b = *(const s16x4*)(qhi + y * TS);
      if (g == 3) a = zz;
      if (g >= 2) b = zz;
      const bf16x8 qf = cat8(a, b);
      f32x4 s0v = mfma16(kc, qf, z4), s1v = mfma16(kn, qf, z4);
#pragma unroll
      for (int r = 0; r < 4; ++r) {
        const bool a0 = (y == 0) ? (m0[r] && !(art == 0 && 4 * g + r < 8)) : m0[r];
        s0v[r] = a0 ? s0v[r] : ninf;
        s1v[r] = m1[r] ? s1v[r] : ninf;
      }
      float mx = fmaxf(__builtin_fmaxf(__builtin_fmaxf(s0v[0], s0v[1]), s0v[2]), __builtin_fmaxf(__builtin_fmaxf(s0v[3], s1v[0]), s1v[1]));
      mx = fmaxf(mx, fmaxf(s1v[2], s1v[3]));
      mx = x16_max(mx);
      const float mc = -mx * QSCALE;
      float p0[4], p1[4];
#pragma unroll
      for (int r = 0; r < 4; ++r) {
        p0[r] = __builtin_amdgcn_exp2f(__builtin_fmaf(s0v[r], QSCALE, mc));
        p1[r] = __builtin_amdgcn_exp2f(__builtin_fmaf(s1v[r], QSCALE, mc));
      }
      const float ls = ((p0[0] + p0[1]) + (p0[2] + p0[3])) + ((p1[0] + p1[1]) + (p1[2] + p1[3]));
      const float inv = __builtin_amdgcn_rcpf(x16_sum(ls));
      bf16x8 pf;
#pragma unroll
      for (int r = 0; r < 4; ++r) { pf[r] = (__bf16)p0[r]; pf[4 + r] = (__bf16)p1[r]; }
      const f32x4 o0 = mfma16(cat8(vc0, vn0), pf, z4);
      const f32x4 o1 = mfma16(cat8(vc1, vn1), pf, z4);
      const int s = art * 128 + 32 * y + 2 * l15 + par;
      char* orow = ws + WS_OA + (size_t)s * 1280 + (2 * hp + hh) * 40;
      st8(orow + g * 8, __builtin_bit_cast(u32x2, pack4(o0[0] * inv, o0[1] * inv, o0[2] * inv, o0[3] * inv)));
      if (g == 0) st8(orow + 32, __builtin_bit_cast(u32x2, pack4(o1[0] * inv, o1[1] * inv, o1[2] * inv, o1[3] * inv)));
      kc = kn; vc0 = vn0; vc1 = vn1;
    }
    return;
  }
  bf16x8 q1a, q1b; s16x4 q2a, q2b;
  prep_q(lds + K2_QI + (pr * 2) * 16, lane, km, q1a, q2a);
  prep_q(lds + K2_QI + (pr * 2 + 1) * 16, lane, km, q1b, q2b);
  f32x16 oa = {}, ob = {};
  {
    u32x4 k1, v0, v1 = {0u, 0u, 0u, 0u}; u32x2 k2;
    unsigned a16, a8, n16, n8;
    const unsigned base16 = lbase + kh * 4 * KV_TILE + lane * 16, base8 = lbase + kh * 4 * KV_TILE + lane * 8;
    unsigned b0 = 0, b1 = K2_STAGE, b2 = 2 * K2_STAGE, st, tmp, t2, fv;
    const unsigned fa = lbase + K2_FL, da = lbase + K2_FL + 256 + w * 256 + lane * 4;
    asm volatile(
      "v_add_u32 %[a16], %[b0], %[base16]\n"
      "v_add_u32 %[a8], %[b0], %[base8]\n"
      "ds_read_b128 %[k1], %[a16]\n"
      "ds_read_b64 %[k2], %[a8] offset:1024\n"
      "ds_read_b128 %[v0], %[a16] offset:1536\n"
      "v_mov_b32 v100, 0\n"
      "v_mov_b32 v101, 0\n"
      "v_mov_b32 v102, 0\n"
      "v_mov_b32 v103, 0\n"
      "s_waitcnt lgkmcnt(0)\n"
      "v_mfma_f32_32x32x16_bf16 v[64:79], %[k1], %[q1a], 0\n"
      "v_mfma_f32_32x32x8_bf16 v[64:79], %[k2], %[q2a], v[64:79]\n"
      "s_mov_b32 %[st], 0\n"
      "s_nop 7\n"
      "s_nop 7\n"
      ".p2align 6\n"
      "s_nop 0\n"
      "1:\n"
      "v_add_u32 %[a16], %[b0], %[base16]\n"
      "v_add_u32 %[a8], %[b0], %[base8]\n"
      "v_add_u32 %[n16], %[b1], %[base16]\n"
      "v_add_u32 %[n8], %[b1], %[base8]\n"
      "v_exp_f32 v104, v64\n"
      "v_exp_f32 v105, v65\n"
      "v_mfma_f32_32x32x16_bf16 %[ob], %[v1], v[100:103], %[ob]\n"
      "ds_read_b128 %[v1], %[a16] offset:2560\n"
      "ds_read_b128 %[v0b], %[a16] offset:5120\n"
      "v_exp_f32 v106, v66\n"
      "v_exp_f32 v107, v67\n"
      "v_cvt_pk_bf16_f32 v96, v104, v105\n"
      "v_cvt_pk_bf16_f32 v97, v106, v107\n"
      "v_exp_f32 v108, v68\n"
      "v_exp_f32 v109, v69\n"
      "v_mfma_f32_32x32x16_bf16 v[80:95], %[k1], %[q1b], 0\n"
      "ds_read_b128 %[k1], %[a16] offset:3584\n"
      "v_exp_f32 v110, v70\n"
      "v_exp_f32 v111, v71\n"
      "v_cvt_pk_bf16_f32 v98, v108, v109\n"
      "v_cvt_pk_bf16_f32 v99, v110, v111\n"
      "v_exp_f32 v104, v72\n"
      "v_exp_f32 v105, v73\n"
      "v_mfma_f32_32x32x8_bf16 v[80:95], %[k2], %[q2b], v[80:95]\n"
      "ds_read_b64 %[k2], %[a8] offset:4608\n"
      "v_exp_f32 v106, v74\n"
      "v_exp_f32 v107, v75\n"
      "v_cvt_pk_bf16_f32 v100, v104, v105\n"
      "v_cvt_pk_bf16_f32 v101, v106, v107\n"
      "v_exp_f32 v108, v76\n"
      "v_exp_f32 v109, v77\n"
      "v_mfma_f32_32x32x16_bf16 %[oa], %[v0], v[96:99], %[oa]\n"
      "v_exp_f32 v110, v78\n"
      "v_exp_f32 v111, v79\n"
      "v_cvt_pk_bf16_f32 v102, v108, v109\n"
      "v_cvt_pk_bf16_f32 v103, v110, v111\n"
      "v_exp_f32 v104, v80\n"
      "v_exp_f32 v105, v81\n"
      "s_waitcnt lgkmcnt(0)\n"
      "v_mfma_f32_32x32x16_bf16 %[oa], %[v1], v[100:103], %[oa]\n"
      "v_exp_f32 v106, v82\n"
      "v_exp_f32 v107, v83\n"
      "v_cvt_pk_bf16_f32 v96, v104, v105\n"
      "v_cvt_pk_bf16_f32 v97, v106, v107\n"
      "v_exp_f32 v108, v84\n"
      "v_exp_f32 v109, v85\n"
      "v_mfma_f32_32x32x16_bf16 v[64:79], %[k1], %[q1a], 0\n"
      "v_exp_f32 v110, v86\n"
      "v_exp_f32 v111, v87\n"
      "v_cvt_pk_bf16_f32 v98, v108, v109\n"
      "v_cvt_pk_bf16_f32 v99, v110, v111\n"
      "v_exp_f32 v104, v88\n"
      "v_exp_f32 v105, v89\n"
      "v_mfma_f32_32x32x8_bf16 v[64:79], %[k2], %[q2a], v[64:79]\n"
      "v_exp_f32 v106, v90\n"
      "v_exp_f32 v107, v91\n"
      "v_cvt_pk_bf16_f32 v100, v104, v105\n"
      "v_cvt_pk_bf16_f32 v101, v106, v107\n"
      "v_exp_f32 v108, v92\n"
      "v_exp_f32 v109, v93\n"
      "v_mfma_f32_32x32x16_bf16 %[ob], %[v0], v[96:99], %[ob]\n"
      "v_exp_f32 v110, v94\n"
      "v_exp_f32 v111, v95\n"
      "v_cvt_pk_bf16_f32 v102, v108, v109\n"
      "v_cvt_pk_bf16_f32 v103, v110, v111\n"
      "v_exp_f32 v104, v64\n"
      "v_exp_f32 v105, v65\n"
      "v_mfma_f32_32x32x16_bf16 %[ob], %[v1], v[100:103], %[ob]\n"
      "ds_read_b128 %[v1], %[a16] offset:6144\n"
      "ds_read_b128 %[v0], %[a16] offset:8704\n"
      "v_exp_f32 v106, v66\n"
      "v_exp_f32 v107, v67\n"
      "v_cvt_pk_bf16_f32 v96, v104, v105\n"
      "v_cvt_pk_bf16_f32 v97, v106, v107\n"
      "v_exp_f32 v108, v68\n"
      "v_exp_f32 v109, v69\n"
      "v_mfma_f32_32x32x16_bf16 v[80:95], %[k1], %[q1b], 0\n"
      "ds_read_b128 %[k1], %[a16] offset:7168\n"
      "v_exp_f32 v110, v70\n"
      "v_exp_f32 v111, v71\n"
      "v_cvt_pk_bf16_f32 v98, v108, v109\n"
      "v_cvt_pk_bf16_f32 v99, v110, v111\n"
      "v_exp_f32 v104, v72\n"
      "v_exp_f32 v105, v73\n"
      "v_mfma_f32_32x32x8_bf16 v[80:95], %[k2], %[q2b], v[80:95]\n"
      "ds_read_b64 %[k2], %[a8] offset:8192\n"
      "v_exp_f32 v106, v74\n"
      "v_exp_f32 v107, v75\n"
      "v_cvt_pk_bf16_f32 v100, v104, v105\n"
      "v_cvt_pk_bf16_f32 v101, v106, v107\n"
      "v_exp_f32 v108, v76\n"
      "v_exp_f32 v109, v77\n"
      "v_mfma_f32_32x32x16_bf16 %[oa], %[v0b], v[96:99], %[oa]\n"
      "v_exp_f32 v110, v78\n"
      "v_exp_f32 v111, v79\n"
      "v_cvt_pk_bf16_f32 v102, v108, v109\n"
      "v_cvt_pk_bf16_f32 v103, v110, v111\n"
      "v_exp_f32 v104, v80\n"
      "v_exp_f32 v105, v81\n"
      "s_waitcnt lgkmcnt(0)\n"
      "v_mfma_f32_32x32x16_bf16 %[oa], %[v1], v[100:103], %[oa]\n"
      "v_exp_f32 v106, v82\n"
      "v_exp_f32 v107, v83\n"
      "v_cvt_pk_bf16_f32 v96, v104, v105\n"
      "v_cvt_pk_bf16_f32 v97, v106, v107\n"
      "v_exp_f32 v108, v84\n"
      "v_exp_f32 v109, v85\n"
      "v_mfma_f32_32x32x16_bf16 v[64:79], %[k1], %[q1a], 0\n"
      "v_exp_f32 v110, v86\n"
      "v_exp_f32 v111, v87\n"
      "v_cvt_pk_bf16_f32 v98, v108, v109\n"
      "v_cvt_pk_bf16_f32 v99, v110, v111\n"
      "v_exp_f32 v104, v88\n"
      "v_exp_f32 v105, v89\n"
      "v_mfma_f32_32x32x8_bf16 v[64:79], %[k2], %[q2a], v[64:79]\n"
      "v_exp_f32 v106, v90\n"
      "v_exp_f32 v107, v91\n"
      "v_cvt_pk_bf16_f32 v100, v104, v105\n"
      "v_cvt_pk_bf16_f32 v101, v106, v107\n"
      "v_exp_f32 v108, v92\n"
      "v_exp_f32 v109, v93\n"
      "v_mfma_f32_32x32x16_bf16 %[ob], %[v0b], v[96:99], %[ob]\n"
      "v_exp_f32 v110, v94\n"
      "v_exp_f32 v111, v95\n"
      "v_cvt_pk_bf16_f32 v102, v108, v109\n"
      "v_cvt_pk_bf16_f32 v103, v110, v111\n"
      "v_exp_f32 v104, v64\n"
      "v_exp_f32 v105, v65\n"
      "v_mfma_f32_32x32x16_bf16 %[ob], %[v1], v[100:103], %[ob]\n"
      "ds_read_b128 %[v1], %[a16] offset:9728\n"
      "ds_read_b128 %[v0b], %[a16] offset:12288\n"
      "v_exp_f32 v106, v66\n"
      "v_exp_f32 v107, v67\n"
      "v_cvt_pk_bf16_f32 v96, v104, v105\n"
      "v_cvt_pk_bf16_f32 v97, v106, v107\n"
      "v_exp_f32 v108, v68\n"
      "v_exp_f32 v109, v69\n"
      "v_mfma_f32_32x32x16_bf16 v[80:95], %[k1], %[q1b], 0\n"
      "ds_read_b128 %[k1], %[a16] offset:10752\n"
      "v_exp_f32 v110, v70\n"
      "v_exp_f32 v111, v71\n"
      "v_cvt_pk_bf16_f32 v98, v108, v109\n"
      "v_cvt_pk_bf16_f32 v99, v110, v111\n"
      "v_exp_f32 v104, v72\n"
      "v_exp_f32 v105, v73\n"
      "v_mfma_f32_32x32x8_bf16 v[80:95], %[k2], %[q2b], v[80:95]\n"
      "ds_read_b64 %[k2], %[a8] offset:11776\n"
      "v_exp_f32 v106, v74\n"
      "v_exp_f32 v107, v75\n"
      "v_cvt_pk_bf16_f32 v100, v104, v105\n"
      "v_cvt_pk_bf16_f32 v101, v106, v107\n"
      "v_exp_f32 v108, v76\n"
      "v_exp_f32 v109, v77\n"
      "v_mfma_f32_32x32x16_bf16 %[oa], %[v0], v[96:99], %[oa]\n"
      "v_exp_f32 v110, v78\n"
      "v_exp_f32 v111, v79\n"
      "v_cvt_pk_bf16_f32 v102, v108, v109\n"
      "v_cvt_pk_bf16_f32 v103, v110, v111\n"
      "v_exp_f32 v104, v80\n"
      "v_exp_f32 v105, v81\n"
      "s_waitcnt lgkmcnt(0)\n"
      "v_mfma_f32_32x32x16_bf16 %[oa], %[v1], v[100:103], %[oa]\n"
      "v_exp_f32 v106, v82\n"
      "v_exp_f32 v107, v83\n"
      "v_cvt_pk_bf16_f32 v96, v104, v105\n"
      "v_cvt_pk_bf16_f32 v97, v106, v107\n"
      "v_exp_f32 v108, v84\n"
      "v_exp_f32 v109, v85\n"
      "v_mfma_f32_32x32x16_bf16 v[64:79], %[k1], %[q1a], 0\n"
      "v_exp_f32 v110, v86\n"
      "v_exp_f32 v111, v87\n"
      "v_cvt_pk_bf16_f32 v98, v108, v109\n"
      "v_cvt_pk_bf16_f32 v99, v110, v111\n"
      "v_exp_f32 v104, v88\n"
      "v_exp_f32 v105, v89\n"
      "v_mfma_f32_32x32x8_bf16 v[64:79], %[k2], %[q2a], v[64:79]\n"
      "v_exp_f32 v106, v90\n"
      "v_exp_f32 v107, v91\n"
      "v_cvt_pk_bf16_f32 v100, v104, v105\n"
      "v_cvt_pk_bf16_f32 v101, v106, v107\n"
      "v_exp_f32 v108, v92\n"
      "v_exp_f32 v109, v93\n"
      "v_mfma_f32_32x32x16_bf16 %[ob], %[v0], v[96:99], %[ob]\n"
      "v_exp_f32 v110, v94\n"
      "v_exp_f32 v111, v95\n"
      "v_cvt_pk_bf16_f32 v102, v108, v109\n"
      "v_cvt_pk_bf16_f32 v103, v110, v111\n"
      "s_cmp_eq_u32 %[st], 7\n"
      "s_cbranch_scc1 2f\n"
      "s_add_u32 %[tmp], %[st], 2\n"
      "3:\n"
      "ds_read_b32 %[fv], %[fa]\n"
      "s_waitcnt lgkmcnt(0)\n"
      "v_readfirstlane_b32 %[t2], %[fv]\n"
      "s_cmp_ge_u32 %[t2], %[tmp]\n"
      "s_cbranch_scc1 2f\n"
      "s_sleep 1\n"
      "s_branch 3b\n"
      "2:\n"
      "v_exp_f32 v104, v64\n"
      "v_exp_f32 v105, v65\n"
      "v_mfma_f32_32x32x16_bf16 %[ob], %[v1], v[100:103], %[ob]\n"
      "ds_read_b128 %[v1], %[a16] offset:13312\n"
      "ds_read_b128 %[v0], %[n16] offset:1536\n"
      "v_exp_f32 v106, v66\n"
      "v_exp_f32 v107, v67\n"
      "v_cvt_pk_bf16_f32 v96, v104, v105\n"
      "v_cvt_pk_bf16_f32 v97, v106, v107\n"
      "v_exp_f32 v108, v68\n"
      "v_exp_f32 v109, v69\n"
      "v_mfma_f32_32x32x16_bf16 v[80:95], %[k1], %[q1b], 0\n"
      "ds_read_b128 %[k1], %[n16]\n"
      "v_exp_f32 v110, v70\n"
      "v_exp_f32 v111, v71\n"
      "v_cvt_pk_bf16_f32 v98, v108, v109\n"
      "v_cvt_pk_bf16_f32 v99, v110, v111\n"
      "v_exp_f32 v104, v72\n"
      "v_exp_f32 v105, v73\n"
      "v_mfma_f32_32x32x8_bf16 v[80:95], %[k2], %[q2b], v[80:95]\n"
      "ds_read_b64 %[k2], %[n8] offset:1024\n"
      "v_exp_f32 v106, v74\n"
      "v_exp_f32 v107, v75\n"
      "v_cvt_pk_bf16_f32 v100, v104, v105\n"
      "v_cvt_pk_bf16_f32 v101, v106, v107\n"
      "v_exp_f32 v108, v76\n"
      "v_exp_f32 v109, v77\n"
      "v_mfma_f32_32x32x16_bf16 %[oa], %[v0b], v[96:99], %[oa]\n"
      "v_exp_f32 v110, v78\n"
      "v_exp_f32 v111, v79\n"
      "v_cvt_pk_bf16_f32 v102, v108, v109\n"
      "v_cvt_pk_bf16_f32 v103, v110, v111\n"
      "v_exp_f32 v104, v80\n"
      "v_exp_f32 v105, v81\n"
      "s_waitcnt lgkmcnt(0)\n"
      "v_mfma_f32_32x32x16_bf16 %[oa], %[v1], v[100:103], %[oa]\n"
      "v_exp_f32 v106, v82\n"
      "v_exp_f32 v107, v83\n"
      "v_cvt_pk_bf16_f32 v96, v104, v105\n"
      "v_cvt_pk_bf16_f32 v97, v106, v107\n"
      "v_exp_f32 v108, v84\n"
      "v_exp_f32 v109, v85\n"
      "v_mfma_f32_32x32x16_bf16 v[64:79], %[k1], %[q1a], 0\n"
      "v_exp_f32 v110, v86\n"
      "v_exp_f32 v111, v87\n"
      "v_cvt_pk_bf16_f32 v98, v108, v109\n"
      "v_cvt_pk_bf16_f32 v99, v110, v111\n"
      "v_exp_f32 v104, v88\n"
      "v_exp_f32 v105, v89\n"
      "v_mfma_f32_32x32x8_bf16 v[64:79], %[k2], %[q2a], v[64:79]\n"
      "v_exp_f32 v106, v90\n"
      "v_exp_f32 v107, v91\n"
      "v_cvt_pk_bf16_f32 v100, v104, v105\n"
      "v_cvt_pk_bf16_f32 v101, v106, v107\n"
      "v_exp_f32 v108, v92\n"
      "v_exp_f32 v109, v93\n"
      "v_mfma_f32_32x32x16_bf16 %[ob], %[v0b], v[96:99], %[ob]\n"
      "v_exp_f32 v110, v94\n"
      "v_exp_f32 v111, v95\n"
      "v_cvt_pk_bf16_f32 v102, v108, v109\n"
      "v_cvt_pk_bf16_f32 v103, v110, v111\n"
      "s_add_u32 %[st], %[st], 1\n"
      "v_mov_b32 %[fv], %[st]\n"
      "ds_write_b32 %[da], %[fv]\n"
      "s_mov_b32 %[tmp], %[b0]\n"
      "s_mov_b32 %[b0], %[b1]\n"
      "s_mov_b32 %[b1], %[b2]\n"
      "s_mov_b32 %[b2], %[tmp]\n"
      "s_cmp_lt_u32 %[st], 8\n"
      "s_cbranch_scc1 1b\n"
      "s_nop 1\n"
      "v_mfma_f32_32x32x16_bf16 %[ob], %[v1], v[100:103], %[ob]\n"
      "s_waitcnt lgkmcnt(0)\n"
      "s_nop 15\n"
      : [oa] "+v"(oa), [ob] "+v"(ob), [k1] "=&v"(k1), [k2] "=&v"(k2), [v0] "=&v"(v0), [v1] "+v"(v1),
        [a16] "=&v"(a16), [a8] "=&v"(a8), [n16] "=&v"(n16), [n8] "=&v"(n8),
        [b0] "+s"(b0), [b1] "+s"(b1), [b2] "+s"(b2), [st] "=&s"(st), [tmp] "=&s"(tmp), [t2] "=&s"(t2), [fv] "=&v"(fv)
      : [q1a] "v"(q1a), [q2a] "v"(q2a), [q1b] "v"(q1b), [q2b] "v"(q2b), [base16] "v"(base16), [base8] "v"(base8),
        [fa] "v"(fa), [da] "v"(da)
      : "memory", "scc", "v64", "v65", "v66", "v67", "v68", "v69", "v70", "v71", "v72", "v73", "v74", "v75", "v76", "v77", "v78", "v79", "v80", "v81", "v82", "v83", "v84", "v85", "v86", "v87", "v88", "v89", "v90", "v91", "v92", "v93", "v94", "v95", "v96", "v97", "v98", "v99", "v100", "v101", "v102", "v103", "v104", "v105", "v106", "v107", "v108", "v109", "v110", "v111");
  }
  float* ex = (float*)(lds + K2_EX + pr * 6144);
  if (kh == 0) {
#pragma unroll
    for (int r4 = 0; r4 < 3; ++r4) {
      *(f32x4*)(ex + r4 * 256 + lane * 4) = f32x4{oa[4 * r4], oa[4 * r4 + 1], oa[4 * r4 + 2], oa[4 * r4 + 3]};
      *(f32x4*)(ex + 768 + r4 * 256 + lane * 4) = f32x4{ob[4 * r4], ob[4 * r4 + 1], ob[4 * r4 + 2], ob[4 * r4 + 3]};
    }
    asm volatile("s_waitcnt lgkmcnt(0)" ::: "memory");
    fl[2 + pr] = 1u;
    return;
  }
  while (fl[2 + pr] == 0u) __builtin_amdgcn_s_sleep(1);
  asm volatile("" ::: "memory");
  const int h = lane & 31, hf = lane >> 5;
  const int s0 = sb + pr * 2;
  char* img = lds + K2_QI + pr * 2560;
#pragma unroll
  for (int a = 0; a < 2; ++a) {
    const f32x16& mine = a ? ob : oa;
    float o[12];
#pragma unroll
    for (int r4 = 0; r4 < 3; ++r4) {
      const f32x4 v = *(const f32x4*)(ex + a * 768 + r4 * 256 + lane * 4);
#pragma unroll
      for (int r = 0; r < 4; ++r) o[4 * r4 + r] = mine[4 * r4 + r] + v[r];
    }
    const int s = s0 + a;
    const float l = x32_sum(hf ? o[8] : 0.f);
    char* orow = img + a * 1280 + h * 40;
    const bool bad = !(l >= 8.6736174e-19f) || !(l <= 3.0e38f);
    if (__builtin_expect(__any(bad), 0)) {
      const char* qb = ws + WS_QP + (size_t)(s >> 5) * 80 * 512 + (s & 31) * 16;
      float q[20], oo[20];
#pragma unroll
      for (int d = 0; d < 20; ++d) { const int col = 20 * h + d; q[d] = bf2f(*(const __bf16*)(qb + (col >> 3) * 512 + (col & 7) * 2)); oo[d] = 0.f; }
      float m = -3.0e38f, ls = 0.f;
#pragma unroll 1
      for (int kk = 0; kk < S_; ++kk) {
        const char* tb = KVf + (size_t)(kk >> 5) * KV_TILE;
        const int kl = kk & 31;
        float sc = 0.f;
#pragma unroll
        for (int d = 0; d < 16; ++d) sc += q[d] * bf2f(*(const __bf16*)(tb + (kl + 32 * (d >> 3)) * 16 + (d & 7) * 2));
#pragma unroll
        for (int d = 16; d < 20; ++d) sc += q[d] * bf2f(*(const __bf16*)(tb + 1024 + kl * 8 + (d - 16) * 2));
        const float mn = fmaxf(m, sc), al = __builtin_amdgcn_exp2f(m - mn), p = __builtin_amdgcn_exp2f(sc - mn);
        const int sx = kl >> 4, k16 = kl & 15, vh = (k16 >> 2) & 1, jj = (k16 & 3) + 4 * (k16 >> 3);
        ls = ls * al + p;
#pragma unroll
        for (int d = 0; d < 20; ++d) oo[d] = oo[d] * al + p * bf2f(*(const __bf16*)(tb + 1536 + sx * 1024 + (d + 32 * vh) * 16 + jj * 2));
        m = mn;
      }
      const float inv = 1.f / ls;
      if (hf == 0) {
#pragma unroll
        for (int d4 = 0; d4 < 5; ++d4) *(bf16x4*)(orow + d4 * 8) = pack4(oo[4 * d4] * inv, oo[4 * d4 + 1] * inv, oo[4 * d4 + 2] * inv, oo[4 * d4 + 3] * inv);
      }
    } else {
      const float inv = __builtin_amdgcn_rcpf(l);
      *(bf16x4*)(orow + hf * 8) = pack4(o[0] * inv, o[1] * inv, o[2] * inv, o[3] * inv);
      *(bf16x4*)(orow + 16 + hf * 8) = pack4(o[4] * inv, o[5] * inv, o[6] * inv, o[7] * inv);
      if (hf == 0) *(bf16x4*)(orow + 32) = pack4(o[8] * inv, o[9] * inv, o[10] * inv, o[11] * inv);
    }
  }
  asm volatile("s_waitcnt lgkmcnt(0)" ::: "memory");
  {
    char* op = ws + WS_OB + (size_t)s0 * 1280 + lane * 16;
    st16(op, *(const u32x4*)(img + lane * 16));
    st16(op + 1024, *(const u32x4*)(img + 1024 + lane * 16));
    if (lane < 32) st16(op + 2048, *(const u32x4*)(img + 2048 + lane * 16));
  }
}

constexpr int K3_XB = 24576;

__global__ __launch_bounds__(512, 2) void k3_out(const __bf16* __restrict__ woA, const __bf16* __restrict__ woB,
                                                 char* __restrict__ ws, __bf16* __restrict__ out) {
  __shared__ __attribute__((aligned(16))) char lds[G_NB * G_STAGE];
  const int tid = threadIdx.x, lane = tid & 63, w = __builtin_amdgcn_readfirstlane(tid >> 6);
  const int T = (blockIdx.x & 7) * 28 + (blockIdx.x >> 3);
  if (T >= 220) return;
  const int rt = (T * 205) >> 11, ct = T - rt * 10;
  const unsigned lbase = (unsigned)(size_t)lds;
  const int l15 = lane & 15, g = lane >> 4;
  if (__builtin_expect(w >= 4, 1)) {
    const int lw = w - 4;
    const unsigned wv0 = (lane >> 3) * 1280 + ((((lane & 7) >> 1) ^ ((lane >> 4) & 1)) << 5) + ((lane & 1) << 4), wv1 = wv0 ^ 64;
    unsigned aoff[6], al[6];
#pragma unroll
    for (int ii = 0; ii < 6; ++ii) {
      int p = lw * 6 + ii + ct; p = p >= 24 ? p - 24 : p;
      const int row = 4 * p + (lane >> 4);
      int s = rt * 96 + row; s = s > S_ - 1 ? S_ - 1 : s;
      aoff[ii] = s * 1280 + (((lane & 15) ^ (row & 15)) << 4);
      al[ii] = p * 1024;
    }
    auto issue = [&](int st, int buf) {
      const int part = st >= 5, kk = st - 5 * part;
      const char* ab = ws + (part ? WS_OB : WS_OA) + kk * 256;
      const char* wp = (const char*)(part ? woB : woA) + (size_t)(kk * 128 + lw * 32) * 1280 + ct * 128;
      const unsigned lb = lbase + buf * G_STAGE;
      const unsigned wl = lb + K3_XB + lw * 4096;
      dma16(ab, aoff[0], lb + al[0]); dma16(wp, wv0, wl); dma16(ab, aoff[1], lb + al[1]);
      dma16(ab, aoff[2], lb + al[2]); dma16(wp + 8 * 1280, wv1, wl + 1024); dma16(ab, aoff[3], lb + al[3]);
      dma16(wp + 16 * 1280, wv0, wl + 2048); dma16(ab, aoff[4], lb + al[4]); dma16(ab, aoff[5], lb + al[5]);
      dma16(wp + 24 * 1280, wv1, wl + 3072);
    };
    issue(0, 0); issue(1, 1);
    WAIT_BAR(10);
    issue(2, 2);
    WAIT_BAR(10);
#pragma unroll 1
    for (int st = 0; st < 9; ++st) {
      if (st < 7) { issue(st + 3, (st + 3) & 3); WAIT_BAR(10); }
      else { WAIT_BAR(0); }
    }
  } else {
    const int wm = w >> 1, wn = w & 1;
    const int p4 = lane & 3, q4 = l15 >> 2;
    const int xo0 = (wm * 48 + l15) * 256 + ((g ^ l15) << 4);
    const int f = ((q4 >> 1) & 1) | ((g & 1) << 1);
    int wb[2];
#pragma unroll
    for (int j = 0; j < 2; ++j) wb[j] = K3_XB + (8 * g + q4) * 128 + 8 * p4 + 32 * ((2 * wn + j) ^ f);
    float gv[3];
#pragma unroll
    for (int mi = 0; mi < 3; ++mi) {
      int s = rt * 96 + wm * 48 + mi * 16 + l15; s = s > S_ - 1 ? S_ - 1 : s;
      const float gg = ((const float*)(ws + WS_G))[s];
      gv[mi] = gg;
    }
    f32x4 acc[3][2] = {}, accA[3][2];
    bf16x8 xf[4][3], wf[4][2];
    auto ldf = [&](int set, const char* sb, int ks) {
#pragma unroll
      for (int mi = 0; mi < 3; ++mi) xf[set][mi] = *(const bf16x8*)(sb + (xo0 ^ (ks * 64)) + mi * 4096);
#pragma unroll
      for (int ni = 0; ni < 2; ++ni) {
        const char* pw = sb + wb[ni] + ks * 32 * 128;
        wf[set][ni] = cat8(trread(pw), trread(pw + 4 * 128));
      }
    };
    auto mm = [&](int set) {
#pragma unroll
      for (int mi = 0; mi < 3; ++mi)
#pragma unroll
        for (int ni = 0; ni < 2; ++ni) acc[mi][ni] = mfma16(wf[set][ni], xf[set][mi], acc[mi][ni]);
#pragma unroll
      for (int i = 0; i < 6; ++i) { __builtin_amdgcn_sched_group_barrier(0x8, 1, 0); __builtin_amdgcn_sched_group_barrier(0x100, 1, 0); }
      __builtin_amdgcn_sched_group_barrier(0x100, 1, 0);
      __builtin_amdgcn_sched_barrier(0);
    };
    WAIT_BAR(10);
    ldf(0, lds, 0); ldf(1, lds, 1);
#pragma unroll
    for (int st = 0; st < 10; ++st) {
      const char* sb = lds + (st & 3) * G_STAGE;
      const char* sn = lds + ((st + 1) & 3) * G_STAGE;
      if (st == 5) {
#pragma unroll
        for (int mi = 0; mi < 3; ++mi)
#pragma unroll
          for (int ni = 0; ni < 2; ++ni) { accA[mi][ni] = acc[mi][ni] * gv[mi]; acc[mi][ni] = f32x4{0.f, 0.f, 0.f, 0.f}; }
      }
      ldf(2, sb, 2); mm(0);
      ldf(3, sb, 3); mm(1);
      if (st == 0) RAWBAR();
      if (st < 9) ldf(0, sn, 0);
      mm(2);
      if (st < 9) ldf(1, sn, 1);
      mm(3);
      if (st < 9) RAWBAR();
    }
#pragma unroll
    for (int mi = 0; mi < 3; ++mi) {
      const u32x4 v = pair_pack(accA[mi][0] + acc[mi][0] * (1.f - gv[mi]), accA[mi][1] + acc[mi][1] * (1.f - gv[mi]));
      *(u32x4*)(lds + (wm * 48 + mi * 16 + l15) * 144 + wn * 64 + (g & 1) * 32 + (g >> 1) * 16) = v;
    }
    BAR();
#pragma unroll
    for (int j = 0; j < 3; ++j) {
      const int row = w * 24 + j * 8 + (lane >> 3), s = rt * 96 + row;
      if (s < S_) st16((char*)out + (size_t)s * 1280 + ct * 128 + (lane & 7) * 16, *(const u32x4*)(lds + row * 144 + (lane & 7) * 16));
    }
  }
}

extern "C" void kernel_launch(void* const* d_in, const int* in_sizes, int n_in, void* d_out, int out_size, void* d_ws,
                              size_t ws_size, hipStream_t stream) {
  const __bf16* x = (const __bf16*)d_in[0];
  const __bf16* wqkv = (const __bf16*)d_in[1];
  const __bf16* woA = (const __bf16*)d_in[2];
  const __bf16* wq = (const __bf16*)d_in[4];
  const __bf16* wk = (const __bf16*)d_in[5];
  const __bf16* wv = (const __bf16*)d_in[6];
  const __bf16* woB = (const __bf16*)d_in[7];
  const __bf16* gw = (const __bf16*)d_in[8];
  char* ws = (char*)d_ws;
  k1_proj<<<256, 768, 0, stream>>>(x, wqkv, wq, wk, wv, gw, ws);
  k2_attn<<<256, 832, 0, stream>>>(ws, (const char*)woA, (const char*)woB);
  k3_out<<<224, 512, 0, stream>>>(woA, woB, ws, (__bf16*)d_out);
}
```

```cpp
#include <hip/hip_runtime.h>
#include <hip/hip_bf16.h>
#include <stdint.h>

#define DI __device__ __forceinline__
typedef __attribute__((ext_vector_type(8))) __bf16 bf16x8;
typedef __attribute__((ext_vector_type(4))) __bf16 bf16x4;
typedef __attribute__((ext_vector_type(4))) short s16x4;
typedef __attribute__((ext_vector_type(8))) short s16x8;
typedef __attribute__((ext_vector_type(16))) float f32x16;
typedef __attribute__((ext_vector_type(4))) float f32x4;
typedef __attribute__((ext_vector_type(4))) unsigned u32x4;
typedef __attribute__((ext_vector_type(2))) unsigned u32x2;
typedef __attribute__((ext_vector_type(16))) unsigned u32x16;
typedef __attribute__((address_space(3))) s16x4* lds_s16x4_ptr;

constexpr int S_ = 2048, D_ = 640;
constexpr float QSCALE = 0.22360679774997896f * 1.4426950408889634f;

constexpr size_t WS_QP = 0;
constexpr size_t WS_QKV = 2048 * 1280;
constexpr size_t WS_KV = WS_QKV + 2048 * 3840;
constexpr size_t WS_G = 13u << 20;
constexpr size_t WS_KMAX = (13u << 20) + 65536;
constexpr size_t WS_OA = 32u << 20;
constexpr size_t WS_OB = 36u << 20;
constexpr int KV_TILE = 3584;

DI f32x4 mfma16(bf16x8 a, bf16x8 b, f32x4 c) { return __builtin_amdgcn_mfma_f32_16x16x32_bf16(a, b, c, 0, 0, 0); }
DI f32x16 mfma32(bf16x8 a, bf16x8 b, f32x16 c) { return __builtin_amdgcn_mfma_f32_32x32x16_bf16(a, b, c, 0, 0, 0); }
DI s16x4 trread(const char* p) { return __builtin_amdgcn_ds_read_tr16_b64_v4i16((lds_s16x4_ptr)p); }
DI bf16x8 cat8(s16x4 lo, s16x4 hi) { return __builtin_bit_cast(bf16x8, __builtin_shufflevector(lo, hi, 0, 1, 2, 3, 4, 5, 6, 7)); }
DI bf16x4 pack4(float a, float b, float c, float d) { bf16x4 r; r[0] = (__bf16)a; r[1] = (__bf16)b; r[2] = (__bf16)c; r[3] = (__bf16)d; return r; }
DI float bf2f(__bf16 v) { return (float)v; }
DI float x16_max(float x) {
  auto s = __builtin_amdgcn_permlane16_swap(__float_as_uint(x), __float_as_uint(x), false, false);
  x = fmaxf(__uint_as_float(s[0]), __uint_as_float(s[1]));
  auto t = __builtin_amdgcn_permlane32_swap(__float_as_uint(x), __float_as_uint(x), false, false);
  return fmaxf(__uint_as_float(t[0]), __uint_as_float(t[1]));
}
DI float x16_sum(float x) {
  auto s = __builtin_amdgcn_permlane16_swap(__float_as_uint(x), __float_as_uint(x), false, false);
  x = __uint_as_float(s[0]) + __uint_as_float(s[1]);
  auto t = __builtin_amdgcn_permlane32_swap(__float_as_uint(x), __float_as_uint(x), false, false);
  return __uint_as_float(t[0]) + __uint_as_float(t[1]);
}
DI float x32_sum(float x) {
  auto t = __builtin_amdgcn_permlane32_swap(__float_as_uint(x), __float_as_uint(x), false, false);
  return __uint_as_float(t[0]) + __uint_as_float(t[1]);
}
template <int CTRL> DI float dppf(float x) { return __builtin_bit_cast(float, __builtin_amdgcn_mov_dpp(__builtin_bit_cast(int, x), CTRL, 0xf, 0xf, true)); }
DI float row16_max(float x) {
  x = fmaxf(x, dppf<0xB1>(x)); x = fmaxf(x, dppf<0x4E>(x)); x = fmaxf(x, dppf<0x141>(x)); x = fmaxf(x, dppf<0x140>(x));
  return x;
}

DI u32x4 pair_pack(const f32x4& a, const f32x4& b) {
  u32x2 pa = __builtin_bit_cast(u32x2, pack4(a[0], a[1], a[2], a[3])), pb = __builtin_bit_cast(u32x2, pack4(b[0], b[1], b[2], b[3]));
  auto s0 = __builtin_amdgcn_permlane16_swap(pa[0], pb[0], false, false);
  auto s1 = __builtin_amdgcn_permlane16_swap(pa[1], pb[1], false, false);
  return u32x4{s0[0], s1[0], s0[1], s1[1]};
}
DI u32x4 quad_pack(const f32x16& a, int q) {
  u32x2 pa = __builtin_bit_cast(u32x2, pack4(a[4 * q], a[4 * q + 1], a[4 * q + 2], a[4 * q + 3]));
  u32x2 pb = __builtin_bit_cast(u32x2, pack4(a[4 * q + 4], a[4 * q + 5], a[4 * q + 6], a[4 * q + 7]));
  auto s0 = __builtin_amdgcn_permlane32_swap(pa[0], pb[0], false, false);
  auto s1 = __builtin_amdgcn_permlane32_swap(pa[1], pb[1], false, false);
  return u32x4{s0[0], s1[0], s0[1], s1[1]};
}
DI void st16(void* p, u32x4 v) { asm volatile("global_store_dwordx4 %0, %1, off sc1\n\ts_nop 1" :: "v"(p), "v"(v) : "memory"); }
DI void st8(void* p, u32x2 v) { asm volatile("global_store_dwordx2 %0, %1, off sc1" :: "v"(p), "v"(v) : "memory"); }
DI const void* uni_ptr(const void* p) {
  const unsigned long long a = (unsigned long long)p;
  const unsigned lo = __builtin_amdgcn_readfirstlane((unsigned)a), hi = __builtin_amdgcn_readfirstlane((unsigned)(a >> 32));
  return (const void*)(((unsigned long long)hi << 32) | lo);
}
DI void dma16(const void* sbase, unsigned voff, unsigned lds_dst) {
  sbase = uni_ptr(sbase); lds_dst = __builtin_amdgcn_readfirstlane(lds_dst);
  unsigned keep;
  asm volatile("s_mov_b32 %0, m0\n\ts_mov_b32 m0, %3\n\ts_nop 2\n\tglobal_load_lds_dwordx4 %1, %2\n\ts_mov_b32 m0, %0"
               : "=&s"(keep) : "v"(voff), "s"(sbase), "s"(lds_dst) : "memory");
}
DI void dma4(const void* sbase, unsigned voff, unsigned lds_dst) {
  sbase = uni_ptr(sbase); lds_dst = __builtin_amdgcn_readfirstlane(lds_dst);
  unsigned keep;
  asm volatile("s_mov_b32 %0, m0\n\ts_mov_b32 m0, %3\n\ts_nop 2\n\tglobal_load_lds_dword %1, %2\n\ts_mov_b32 m0, %0"
               : "=&s"(keep) : "v"(voff), "s"(sbase), "s"(lds_dst) : "memory");
}
#define WAIT_BAR(n) asm volatile("s_waitcnt vmcnt(" #n ") lgkmcnt(0)\n\ts_barrier" ::: "memory")
#define BAR() asm volatile("s_waitcnt lgkmcnt(0)\n\ts_barrier" ::: "memory")
#define RAWBAR() asm volatile("s_barrier" ::: "memory")

constexpr int G_STAGE = 40960, G_NB = 4;
constexpr int K1_XB = 16384;

DI void k1_kv_block(char* lds, int kvb, int w, int lane, const __bf16* x, const __bf16* wk, const __bf16* wv,
                    const __bf16* gw, char* KVf, float* gout, float* kmax2) {
  const unsigned lbase = (unsigned)(size_t)lds;
  const int tid = w * 64 + lane, l15 = lane & 15, g = lane >> 4;
  constexpr int WK = 81920, WV = WK + 25600, GW = WV + 25600;
  {
    const char* xb = (const char*)x + (size_t)kvb * 64 * 1280;
#pragma unroll 2
    for (int p = w; p < 80; p += 12) {
      const int q = p * 64 + lane, row = q / 80, cp = q - row * 80;
      dma16(xb, row * 1280 + (((cp & ~7) | ((cp ^ row) & 7)) << 4), lbase + p * 1024);
    }
    for (int p = w; p < 50; p += 12) {
      const char* src = (p < 25) ? (const char*)wk + p * 1024 : (const char*)wv + (p - 25) * 1024;
      dma16(src, lane * 16, lbase + WK + p * 1024);
    }
    for (int k = tid; k < 640; k += 768) {
      bf16x4 v; v[0] = gw[k]; v[1] = (__bf16)0.f; v[2] = (__bf16)0.f; v[3] = (__bf16)0.f;
      *(bf16x4*)(lds + GW + k * 40) = v;
    }
  }
  WAIT_BAR(0);
  f32x4 acc[3] = {};
  const int row = (w & 3) * 16 + l15;
  if (w < 4) {
    const int p4 = lane & 3, q4 = l15 >> 2;
    int wa[3];
#pragma unroll
    for (int ni = 0; ni < 3; ++ni) {
      const int cg = 4 * ni + p4;
      wa[ni] = (cg < 5 ? WK + cg * 8 : (cg < 10 ? WV + (cg - 5) * 8 : GW)) + (8 * g + q4) * 40;
    }
    const int xo = row * 1280 + ((g ^ (row & 7)) << 4);
#pragma unroll
    for (int ks = 0; ks < 20; ++ks) {
      const bf16x8 xf = *(const bf16x8*)(lds + (xo ^ ((ks & 1) * 64)) + (ks >> 1) * 128);
#pragma unroll
      for (int ni = 0; ni < 3; ++ni) {
        const char* pw = lds + wa[ni] + ks * 32 * 40;
        acc[ni] = mfma16(cat8(trread(pw), trread(pw + 160)), xf, acc[ni]);
      }
    }
  }
  BAR();
  if (w < 4) {
#pragma unroll
    for (int ni = 0; ni < 3; ++ni) {
      const int cg = 4 * ni + g;
      const bf16x4 v = pack4(acc[ni][0], acc[ni][1], acc[ni][2], acc[ni][3]);
      if (cg < 5) *(bf16x4*)(lds + row * 96 + cg * 8) = v;
      else if (cg < 10) *(bf16x4*)(lds + row * 96 + 48 + (cg - 5) * 8) = v;
      else if (cg == 10) gout[kvb * 64 + row] = 1.f / (1.f + __expf(-acc[ni][0]));
    }
  }
  BAR();
  if (w >= 2) return;
  const char* T = lds + w * 32 * 96;
  const int t = kvb * 2 + w;
  char* dst = KVf + (size_t)t * KV_TILE;
  const int key = lane & 31, hf = lane >> 5;
  const u32x4 k1 = *(const u32x4*)(T + key * 96 + hf * 16);
  u32x2 k2 = *(const u32x2*)(T + key * 96 + 32);
  {
    const bf16x8 a = __builtin_bit_cast(bf16x8, k1); const bf16x4 b = __builtin_bit_cast(bf16x4, k2);
    float n = 0.f;
#pragma unroll
    for (int j = 0; j < 8; ++j) n += bf2f(a[j]) * bf2f(a[j]);
    float n3 = 0.f;
#pragma unroll
    for (int j = 0; j < 4; ++j) n3 += bf2f(b[j]) * bf2f(b[j]);
    n = x32_sum(n) + n3;
    n = x16_max(row16_max(n));
    if (lane == 0) kmax2[t] = n;
  }
  st16(dst + lane * 16, k1);
  if (hf) { k2[0] = 0x3F80u; k2[1] = 0u; }
  st8(dst + 1024 + lane * 8, k2);
#pragma unroll
  for (int s = 0; s < 2; ++s) {
    s16x8 v;
#pragma unroll
    for (int j = 0; j < 8; ++j) {
      const int kk = 16 * s + 8 * (j >> 2) + 4 * hf + (j & 3);
      const short e = *(const short*)(T + kk * 96 + 48 + (key < 20 ? key : 0) * 2);
      v[j] = key < 20 ? e : (key == 20 ? (short)0x3F80 : (short)0);
    }
    st16(dst + 1536 + s * 1024 + lane * 16, __builtin_bit_cast(u32x4, v));
  }
}

__global__ __launch_bounds__(768, 3) void k1_proj(const __bf16* __restrict__ x, const __bf16* __restrict__ wqkv,
                                                  const __bf16* __restrict__ wq, const __bf16* __restrict__ wk,
                                                  const __bf16* __restrict__ wv, const __bf16* __restrict__ gw,
                                                  char* __restrict__ ws) {
  __shared__ __attribute__((aligned(16))) char lds[G_NB * G_STAGE];
  const int tid = threadIdx.x, lane = tid & 63, w = __builtin_amdgcn_readfirstlane(tid >> 6);
  const int b = blockIdx.x, xcd = b & 7, idx = b >> 3;
  if (__builtin_expect(idx >= 28, 0)) {
    k1_kv_block(lds, xcd * 4 + (idx - 28), w, lane, x, wk, wv, gw, ws + WS_KV, (float*)(ws + WS_G), (float*)(ws + WS_KMAX));
    return;
  }
  const int rl = (idx * 37) >> 8, cl7 = idx - rl * 7;
  const int rt = 4 * (xcd >> 1) + rl, ct = 7 * (xcd & 1) + cl7;
  const unsigned lbase = (unsigned)(size_t)lds;
  const int l15 = lane & 15, g = lane >> 4;
  if (__builtin_expect(w >= 8, 1)) {
    const int lw = w - 8;
    const int rX = cl7, rW = (0x5320 >> (4 * rl)) & 15;
    const char* xb0 = (const char*)x + (size_t)rt * 128 * 1280;
    const unsigned xe = (lane >> 3) * 1280 + (((lane & 7) ^ (lane >> 4)) << 4);
    const unsigned xv0 = xe ^ ((rX & 1) << 6), xv1 = xv0 ^ 64;
    const bool isq = ct >= 10;
    const unsigned wstride = isq ? 1280u : 3840u;
    const char* wb0 = (isq ? (const char*)wq + (ct - 10) * 384 : (const char*)wqkv + ct * 384);
    const unsigned wv0 = (lane >> 3) * wstride + ((((lane & 7) >> 1) ^ ((lane >> 4) & 1)) << 5) + ((lane & 1) << 4);
    auto issue = [&](int st, int buf) {
      const char* xb = xb0 + st * 128;
      const char* wp = wb0 + (size_t)st * 64 * wstride;
      const unsigned lb = lbase + buf * G_STAGE;
      auto xp = [&](int ii) { const int p = (lw * 4 + ii + rX) & 15; dma16(xb + p * 8 * 1280, (ii & 1) ? xv1 : xv0, lb + p * 1024); };
      auto wpc = [&](int ii) {
        int p = lw * 6 + ii + rW; p = p >= 24 ? p - 24 : p;
        const int ko = (p * 43) >> 7, th = p - ko * 3;
        dma16(wp + (size_t)ko * 8 * wstride + (ct == 13 ? 0 : th * 128), wv0 ^ ((ko & 1) << 6), lb + K1_XB + p * 1024);
      };
      xp(0); wpc(0); wpc(1); xp(1); wpc(2); xp(2); wpc(3); wpc(4); xp(3); wpc(5);
    };
    issue(0, 0); issue(1, 1);
    WAIT_BAR(10);
    issue(2, 2);
    WAIT_BAR(10);
#pragma unroll 1
    for (int st = 0; st < 9; ++st) {
      if (st < 7) { issue(st + 3, (st + 3) & 3); WAIT_BAR(10); }
      else { WAIT_BAR(0); }
    }
  } else {
    const int wm = w >> 2, wn = w & 3;
    if (ct == 13 && wn >= 2) {
#pragma unroll 1
      for (int i = 0; i < 11; ++i) RAWBAR();
      return;
    }
    const int p4 = lane & 3, q4 = l15 >> 2;
    const int xo0 = (wm * 64 + l15) * 128 + ((g ^ ((l15 >> 1) & 7)) << 4);
    int wo[3];
    {
      const int f = ((q4 >> 1) & 1) | ((g & 1) << 1);
#pragma unroll
      for (int ni = 0; ni < 3; ++ni) { const int bb = 3 * wn + ni; wo[ni] = K1_XB + (g * 3 + (bb >> 2)) * 1024 + q4 * 128 + (((bb & 3) ^ f) << 5) + 8 * p4; }
    }
    f32x4 acc[4][3] = {};
    bf16x8 xa[4], wa[3], xb[4], wc[3];
    auto ldf = [&](bf16x8 (&xf)[4], bf16x8 (&wf)[3], const char* sb, int ks) {
#pragma unroll
      for (int mi = 0; mi < 4; ++mi) xf[mi] = *(const bf16x8*)(sb + (xo0 ^ (ks * 64)) + mi * 2048);
#pragma unroll
      for (int ni = 0; ni < 3; ++ni) {
        const char* pw = sb + wo[ni] + ks * 12 * 1024;
        wf[ni] = cat8(trread(pw), trread(pw + 512));
      }
    };
    auto mm = [&](const bf16x8 (&xf)[4], const bf16x8 (&wf)[3]) {
#pragma unroll
      for (int mi = 0; mi < 4; ++mi)
#pragma unroll
        for (int ni = 0; ni < 3; ++ni) acc[mi][ni] = mfma16(wf[ni], xf[mi], acc[mi][ni]);
#pragma unroll
      for (int i = 0; i < 10; ++i) { __builtin_amdgcn_sched_group_barrier(0x8, 1, 0); __builtin_amdgcn_sched_group_barrier(0x100, 1, 0); }
      __builtin_amdgcn_sched_group_barrier(0x8, 2, 0);
      __builtin_amdgcn_sched_barrier(0);
    };
    RAWBAR();
    ldf(xa, wa, lds, 0);
#pragma unroll
    for (int st = 0; st < 10; ++st) {
      const char* sb = lds + (st & 3) * G_STAGE;
      const char* sn = lds + ((st + 1) & 3) * G_STAGE;
      ldf(xb, wc, sb, 1); mm(xa, wa);
      if (st == 0) RAWBAR();
      if (st < 9) ldf(xa, wa, sn, 0);
      mm(xb, wc);
      if (st < 9) RAWBAR();
    }
    char* dst; int no, oc0;
    if (ct >= 10) {
      dst = ws + WS_QP; no = 80; oc0 = (ct - 10) * 24;
#pragma unroll
      for (int mi = 0; mi < 4; ++mi)
#pragma unroll
        for (int ni = 0; ni < 3; ++ni) acc[mi][ni] *= QSCALE;
    } else { dst = ws + WS_QKV; no = 240; oc0 = ct * 24; }
    oc0 += wn * 6;
    dst += (size_t)((rt * 4 + wm * 2) * no) * 512 + l15 * 16;
    const int ocp = oc0 + (g & 1) * 2 + (g >> 1), ocs = oc0 + 4 + (g >> 1);
#pragma unroll
    for (int mi = 0; mi < 4; ++mi) {
      char* d = dst + (size_t)(mi >> 1) * no * 512 + (mi & 1) * 256;
      const u32x4 v = pair_pack(acc[mi][0], acc[mi][1]);
      if (ocp < no) st16(d + (size_t)ocp * 512, v);
      if (ocs < no) st8(d + (size_t)ocs * 512 + (g & 1) * 8, __builtin_bit_cast(u32x2, pack4(acc[mi][2][0], acc[mi][2][1], acc[mi][2][2], acc[mi][2][3])));
    }
  }
}

constexpr int K2_STAGE = 8 * KV_TILE;
constexpr int K2_RA = 3 * K2_STAGE;
constexpr int K2_QI = K2_RA + 38 * 1024;
constexpr int K2_EX = K2_QI + 10400;
constexpr int K2_FL = K2_EX + 8 * 3072;
constexpr int K2_SCR = K2_FL + 2304;

DI float dot2bf(unsigned a, float c) {
  typedef __attribute__((ext_vector_type(2))) __bf16 bf16x2;
  return __builtin_amdgcn_fdot2_f32_bf16(__builtin_bit_cast(bf16x2, a), __builtin_bit_cast(bf16x2, a), c, false);
}
DI void prep_q(const char* qi, int lane, float km, bf16x8& q1, s16x4& q2) {
  const int h = lane & 31, hf = lane >> 5;
  auto qa = [&](int col) { return *(const u32x2*)(qi + (col >> 3) * 128 + (col >> 6) * 16 + (col & 7) * 2); };
  const u32x2 lo = qa(20 * h + 8 * hf), hi = qa(20 * h + 8 * hf + 4), tl = qa(20 * h + 16);
  q1 = __builtin_bit_cast(bf16x8, u32x4{lo[0], lo[1], hi[0], hi[1]});
  const float n2 = x32_sum(dot2bf(lo[0], dot2bf(lo[1], dot2bf(hi[0], dot2bf(hi[1], 0.f))))) + dot2bf(tl[0], dot2bf(tl[1], 0.f));
  const float off = __builtin_amdgcn_sqrtf(n2 * km) * 1.01f - 80.f;
  unsigned ob = __float_as_uint(off);
  ob = (off > 0.f) ? ((ob + 0xFFFFu) & 0xFFFF0000u) : (ob & 0xFFFF0000u);
  u32x2 t2 = tl;
  if (hf) { t2[0] = (ob >> 16) ^ 0x8000u; t2[1] = 0u; }
  q2 = __builtin_bit_cast(s16x4, t2);
}

__global__ __launch_bounds__(832, 4) void k2_attn(char* __restrict__ ws, const char* __restrict__ woA, const char* __restrict__ woB) {
  __shared__ __attribute__((aligned(16))) char lds[K2_SCR + 256];
  const int tid = threadIdx.x, lane = tid & 63, w = __builtin_amdgcn_readfirstlane(tid >> 6);
  const unsigned lbase = (unsigned)(size_t)lds;
  const char* KVf = ws + WS_KV;
  volatile __attribute__((address_space(3))) unsigned* fl = (volatile __attribute__((address_space(3))) unsigned*)(lds + K2_FL);
  const int pr = w & 3, kh = (w >> 2) & 1;
  const int sb = blockIdx.x * 8;
  const int hp = blockIdx.x & 15, art = blockIdx.x >> 4;
  {
    const char* qp = ws + WS_QP + (size_t)(sb >> 5) * 80 * 512 + (sb & 31) * 16;
    const unsigned qv = (lane >> 3) * 512 + (lane & 7) * 16;
#pragma unroll
    for (int i = 0; i < 3; ++i) {
      const int q = w + 13 * i;
      if (q < 10) dma16(qp + q * 4096, qv, lbase + K2_QI + q * 1040);
      else if (q < 38) dma16(KVf + (q - 10) * 1024, lane * 16, lbase + (q - 10) * 1024);
    }
    if (w >= 8) for (int i = tid - 512; i < 576; i += 320) fl[i] = i == 0 ? 1u : 0u;
  }
  float km = ((const float*)(ws + WS_KMAX))[lane];
  km = x16_max(row16_max(km));
  asm volatile("" : "+v"(km));
  WAIT_BAR(0);
  if (w == 8) {
    __builtin_amdgcn_s_setprio(3);
    const unsigned dv = lane * 16;
#pragma unroll 1
    for (int st = 1; st < 8; ++st) {
      if (st >= 3) {
        while (true) {
          const unsigned d = fl[64 + 64 * (lane & 7)];
          if (__all(d >= (unsigned)(st - 2))) break;
          __builtin_amdgcn_s_sleep(2);
        }
        asm volatile("" ::: "memory");
      }
      const char* src = KVf + st * K2_STAGE;
      const unsigned ld = lbase + (st % 3) * K2_STAGE;
#pragma unroll 4
      for (int k = 0; k < 28; ++k) dma16(src + k * 1024, dv, ld + k * 1024);
      asm volatile("s_waitcnt vmcnt(0)" ::: "memory");
      fl[0] = st + 1;
      if (st == 4) {
        const int grp = blockIdx.x >> 3;
#pragma unroll
        for (int k = 0; k < 7; ++k) {
          int ln = k * 64 + lane; ln = ln > 399 ? 399 : ln;
          dma4(grp >= 16 ? woB : woA, ((grp & 15) * 400 + ln) * 128, lbase + K2_SCR);
        }
      }
    }
    return;
  }
  if (w > 8) {
    {
      const char* rap = ws + WS_QKV + ((long)art * 4 - 1) * 240 * 512 + hp * 5 * 512;
      unsigned rp[3];
#pragma unroll
      for (int t = 0; t < 3; ++t) { const int r = 16 + 64 * t + lane; rp[t] = (r >> 5) * 240 * 512 + (r & 31) * 16; }
#pragma unroll 1
      for (int co = w - 9; co < 15; co += 4) {
        const int seg = (co * 13) >> 6;
        const char* src = rap + (seg * 75 + co) * 512;
        const unsigned ld = lbase + K2_RA + co * 2560;
        dma16(src, rp[0], ld); dma16(src, rp[1], ld + 1024);
        if (lane < 32) dma16(src, rp[2], ld + 2048);
      }
      asm volatile("s_waitcnt vmcnt(0)" ::: "memory");
      if (lane == 0) __hip_atomic_fetch_add((__attribute__((address_space(3))) unsigned*)(lds + K2_FL + 4), 1u, __ATOMIC_RELAXED, __HIP_MEMORY_SCOPE_WORKGROUP);
    }
    while (fl[1] < 4u) __builtin_amdgcn_s_sleep(8);
    asm volatile("" ::: "memory");
    const int l15 = lane & 15, g = lane >> 4, p4 = lane & 3, q4 = l15 >> 2;
    const int hh = w & 1, par = ((w - 9) >> 1) & 1;
    constexpr int TS = 4 * 128;
    const char* RA = lds + K2_RA;
    auto ea = [&](int u, int cc) { const int i = 2 * u + par; return RA + ((cc >> 3) * 20 + (i >> 3)) * 128 + (i & 7) * 16 + (cc & 7) * 2; };
    const int gk = g < 3 ? 8 * g : 0;
    const char* klo = ea(l15, 40 + 20 * hh + gk);
    const char* khi = ea(l15, 40 + 20 * hh + gk + 4);
    const char* vp0 = ea(4 * g + q4, 80 + 20 * hh + 4 * p4);
    const char* vp1 = ea(4 * g + q4, 80 + 20 * hh + 16 + (hh == 1 && p4 > 0 ? 0 : 4 * p4));
    const char* qlo = ea(8 + l15, 20 * hh + gk);
    const char* qhi = ea(8 + l15, 20 * hh + gk + 4);
    bool m0[4], m1[4];
#pragma unroll
    for (int r = 0; r < 4; ++r) { const int j = 4 * g + r; m0[r] = (j >= l15 + 1) && (j <= l15 + 8); m1[r] = (j <= l15 - 8); }
          const f32x4 z4 = {0.f, 0.f, 0.f, 0.f};
    const s16x4 zz = {0, 0, 0, 0};
    const float ninf = -__builtin_inff();
    bf16x8 kc = cat8(*(const s16x4*)klo, *(const s16x4*)khi);
    s16x4 vc0 = trread(vp0), vc1 = trread(vp1);
#pragma unroll
    for (int y = 0; y < 4; ++y) {
      const bf16x8 kn = cat8(*(const s16x4*)(klo + (y + 1) * TS), *(const s16x4*)(khi + (y + 1) * TS));
      const s16x4 vn0 = trread(vp0 + (y + 1) * TS), vn1 = trread(vp1 + (y + 1) * TS);
      s16x4 a = *(const s16x4*)(qlo + y * TS), b = *(const s16x4*)(qhi + y * TS);
      if (g == 3) a = zz;
      if (g >= 2) b = zz;
      const bf16x8 qf = cat8(a, b);
      f32x4 s0v = mfma16(kc, qf, z4), s1v = mfma16(kn, qf, z4);
#pragma unroll
      for (int r = 0; r < 4; ++r) {
        const bool a0 = (y == 0) ? (m0[r] && !(art == 0 && 4 * g + r < 8)) : m0[r];
        s0v[r] = a0 ? s0v[r] : ninf;
        s1v[r] = m1[r] ? s1v[r] : ninf;
      }
      float mx = fmaxf(__builtin_fmaxf(__builtin_fmaxf(s0v[0], s0v[1]), s0v[2]), __builtin_fmaxf(__builtin_fmaxf(s0v[3], s1v[0]), s1v[1]));
      mx = fmaxf(mx, fmaxf(s1v[2], s1v[3]));
      mx = x16_max(mx);
      const float mc = -mx * QSCALE;
      float p0[4], p1[4];
#pragma unroll
      for (int r = 0; r < 4; ++r) {
        p0[r] = __builtin_amdgcn_exp2f(__builtin_fmaf(s0v[r], QSCALE, mc));
        p1[r] = __builtin_amdgcn_exp2f(__builtin_fmaf(s1v[r], QSCALE, mc));
      }
      const float ls = ((p0[0] + p0[1]) + (p0[2] + p0[3])) + ((p1[0] + p1[1]) + (p1[2] + p1[3]));
      const float inv = __builtin_amdgcn_rcpf(x16_sum(ls));
      bf16x8 pf;
#pragma unroll
      for (int r = 0; r < 4; ++r) { pf[r] = (__bf16)p0[r]; pf[4 + r] = (__bf16)p1[r]; }
      const f32x4 o0 = mfma16(cat8(vc0, vn0), pf, z4);
      const f32x4 o1 = mfma16(cat8(vc1, vn1), pf, z4);
      const int s = art * 128 + 32 * y + 2 * l15 + par;
      char* orow = ws + WS_OA + (size_t)s * 1280 + (2 * hp + hh) * 40;
      st8(orow + g * 8, __builtin_bit_cast(u32x2, pack4(o0[0] * inv, o0[1] * inv, o0[2] * inv, o0[3] * inv)));
      if (g == 0) st8(orow + 32, __builtin_bit_cast(u32x2, pack4(o1[0] * inv, o1[1] * inv, o1[2] * inv, o1[3] * inv)));
      kc = kn; vc0 = vn0; vc1 = vn1;
    }
    return;
  }
  bf16x8 q1a, q1b; s16x4 q2a, q2b;
  prep_q(lds + K2_QI + (pr * 2) * 16, lane, km, q1a, q2a);
  prep_q(lds + K2_QI + (pr * 2 + 1) * 16, lane, km, q1b, q2b);
  f32x16 oa = {}, ob = {};
  {
    u32x4 k1, v0, v1 = {0u, 0u, 0u, 0u}; u32x2 k2;
    unsigned a16, a8, n16, n8;
    const unsigned base16 = lbase + kh * 4 * KV_TILE + lane * 16, base8 = lbase + kh * 4 * KV_TILE + lane * 8;
    unsigned b0 = 0, b1 = K2_STAGE, b2 = 2 * K2_STAGE, st, tmp, t2, fv;
    const unsigned fa = lbase + K2_FL, da = lbase + K2_FL + 256 + w * 256 + lane * 4;
    asm volatile(
      "v_add_u32 %[a16], %[b0], %[base16]\n"
      "v_add_u32 %[a8], %[b0], %[base8]\n"
      "ds_read_b128 %[k1], %[a16]\n"
      "ds_read_b64 %[k2], %[a8] offset:1024\n"
      "ds_read_b128 %[v0], %[a16] offset:1536\n"
      "v_mov_b32 v100, 0\n"
      "v_mov_b32 v101, 0\n"
      "v_mov_b32 v102, 0\n"
      "v_mov_b32 v103, 0\n"
      "s_waitcnt lgkmcnt(0)\n"
      "v_mfma_f32_32x32x16_bf16 v[64:79], %[k1], %[q1a], 0\n"
      "v_mfma_f32_32x32x8_bf16 v[64:79], %[k2], %[q2a], v[64:79]\n"
      "s_mov_b32 %[st], 0\n"
      "s_nop 7\n"
      "s_nop 7\n"
      ".p2align 6\n"
      "s_nop 0\n"
      "1:\n"
      "v_add_u32 %[a16], %[b0], %[base16]\n"
      "v_add_u32 %[a8], %[b0], %[base8]\n"
      "v_add_u32 %[n16], %[b1], %[base16]\n"
      "v_add_u32 %[n8], %[b1], %[base8]\n"
      "v_exp_f32 v104, v64\n"
      "v_exp_f32 v105, v65\n"
      "v_mfma_f32_32x32x16_bf16 %[ob], %[v1], v[100:103], %[ob]\n"
      "ds_read_b128 %[v1], %[a16] offset:2560\n"
      "v_exp_f32 v106, v66\n"
      "v_exp_f32 v107, v67\n"
      "v_cvt_pk_bf16_f32 v96, v104, v105\n"
      "v_cvt_pk_bf16_f32 v97, v106, v107\n"
      "v_exp_f32 v108, v68\n"
      "v_exp_f32 v109, v69\n"
      "v_mfma_f32_32x32x16_bf16 v[80:95], %[k1], %[q1b], 0\n"
      "v_exp_f32 v110, v70\n"
      "v_exp_f32 v111, v71\n"
      "v_cvt_pk_bf16_f32 v98, v108, v109\n"
      "v_cvt_pk_bf16_f32 v99, v110, v111\n"
      "v_exp_f32 v104, v72\n"
      "v_exp_f32 v105, v73\n"
      "v_mfma_f32_32x32x8_bf16 v[80:95], %[k2], %[q2b], v[80:95]\n"
      "ds_read_b128 %[k1], %[a16] offset:3584\n"
      "ds_read_b64 %[k2], %[a8] offset:4608\n"
      "v_exp_f32 v106, v74\n"
      "v_exp_f32 v107, v75\n"
      "v_cvt_pk_bf16_f32 v100, v104, v105\n"
      "v_cvt_pk_bf16_f32 v101, v106, v107\n"
      "v_exp_f32 v108, v76\n"
      "v_exp_f32 v109, v77\n"
      "s_waitcnt lgkmcnt(3)\n"
      "v_mfma_f32_32x32x16_bf16 %[oa], %[v0], v[96:99], %[oa]\n"
      "v_exp_f32 v110, v78\n"
      "v_exp_f32 v111, v79\n"
      "v_cvt_pk_bf16_f32 v102, v108, v109\n"
      "v_cvt_pk_bf16_f32 v103, v110, v111\n"
      "v_exp_f32 v104, v80\n"
      "v_exp_f32 v105, v81\n"
      "s_waitcnt lgkmcnt(2)\n"
      "v_mfma_f32_32x32x16_bf16 %[oa], %[v1], v[100:103], %[oa]\n"
      "v_exp_f32 v106, v82\n"
      "v_exp_f32 v107, v83\n"
      "v_cvt_pk_bf16_f32 v96, v104, v105\n"
      "v_cvt_pk_bf16_f32 v97, v106, v107\n"
      "v_exp_f32 v108, v84\n"
      "v_exp_f32 v109, v85\n"
      "s_waitcnt lgkmcnt(1)\n"
      "v_mfma_f32_32x32x16_bf16 v[64:79], %[k1], %[q1a], 0\n"
      "v_exp_f32 v110, v86\n"
      "v_exp_f32 v111, v87\n"
      "v_cvt_pk_bf16_f32 v98, v108, v109\n"
      "v_cvt_pk_bf16_f32 v99, v110, v111\n"
      "v_exp_f32 v104, v88\n"
      "v_exp_f32 v105, v89\n"
      "s_waitcnt lgkmcnt(0)\n"
      "v_mfma_f32_32x32x8_bf16 v[64:79], %[k2], %[q2a], v[64:79]\n"
      "v_exp_f32 v106, v90\n"
      "v_exp_f32 v107, v91\n"
      "v_cvt_pk_bf16_f32 v100, v104, v105\n"
      "v_cvt_pk_bf16_f32 v101, v106, v107\n"
      "v_exp_f32 v108, v92\n"
      "v_exp_f32 v109, v93\n"
      "v_mfma_f32_32x32x16_bf16 %[ob], %[v0], v[96:99], %[ob]\n"
      "ds_read_b128 %[v0], %[a16] offset:5120\n"
      "v_exp_f32 v110, v94\n"
      "v_exp_f32 v111, v95\n"
      "v_cvt_pk_bf16_f32 v102, v108, v109\n"
      "v_cvt_pk_bf16_f32 v103, v110, v111\n"
      "v_exp_f32 v104, v64\n"
      "v_exp_f32 v105, v65\n"
      "v_mfma_f32_32x32x16_bf16 %[ob], %[v1], v[100:103], %[ob]\n"
      "ds_read_b128 %[v1], %[a16] offset:6144\n"
      "v_exp_f32 v106, v66\n"
      "v_exp_f32 v107, v67\n"
      "v_cvt_pk_bf16_f32 v96, v104, v105\n"
      "v_cvt_pk_bf16_f32 v97, v106, v107\n"
      "v_exp_f32 v108, v68\n"
      "v_exp_f32 v109, v69\n"
      "v_mfma_f32_32x32x16_bf16 v[80:95], %[k1], %[q1b], 0\n"
      "v_exp_f32 v110, v70\n"
      "v_exp_f32 v111, v71\n"
      "v_cvt_pk_bf16_f32 v98, v108, v109\n"
      "v_cvt_pk_bf16_f32 v99, v110, v111\n"
      "v_exp_f32 v104, v72\n"
      "v_exp_f32 v105, v73\n"
      "v_mfma_f32_32x32x8_bf16 v[80:95], %[k2], %[q2b], v[80:95]\n"
      "ds_read_b128 %[k1], %[a16] offset:7168\n"
      "ds_read_b64 %[k2], %[a8] offset:8192\n"
      "v_exp_f32 v106, v74\n"
      "v_exp_f32 v107, v75\n"
      "v_cvt_pk_bf16_f32 v100, v104, v105\n"
      "v_cvt_pk_bf16_f32 v101, v106, v107\n"
      "v_exp_f32 v108, v76\n"
      "v_exp_f32 v109, v77\n"
      "s_waitcnt lgkmcnt(3)\n"
      "v_mfma_f32_32x32x16_bf16 %[oa], %[v0], v[96:99], %[oa]\n"
      "v_exp_f32 v110, v78\n"
      "v_exp_f32 v111, v79\n"
      "v_cvt_pk_bf16_f32 v102, v108, v109\n"
      "v_cvt_pk_bf16_f32 v103, v110, v111\n"
      "v_exp_f32 v104, v80\n"
      "v_exp_f32 v105, v81\n"
      "s_waitcnt lgkmcnt(2)\n"
      "v_mfma_f32_32x32x16_bf16 %[oa], %[v1], v[100:103], %[oa]\n"
      "v_exp_f32 v106, v82\n"
      "v_exp_f32 v107, v83\n"
      "v_cvt_pk_bf16_f32 v96, v104, v105\n"
      "v_cvt_pk_bf16_f32 v97, v106, v107\n"
      "v_exp_f32 v108, v84\n"
      "v_exp_f32 v109, v85\n"
      "s_waitcnt lgkmcnt(1)\n"
      "v_mfma_f32_32x32x16_bf16 v[64:79], %[k1], %[q1a], 0\n"
      "v_exp_f32 v110, v86\n"
      "v_exp_f32 v111, v87\n"
      "v_cvt_pk_bf16_f32 v98, v108, v109\n"
      "v_cvt_pk_bf16_f32 v99, v110, v111\n"
      "v_exp_f32 v104, v88\n"
      "v_exp_f32 v105, v89\n"
      "s_waitcnt lgkmcnt(0)\n"
      "v_mfma_f32_32x32x8_bf16 v[64:79], %[k2], %[q2a], v[64:79]\n"
      "v_exp_f32 v106, v90\n"
      "v_exp_f32 v107, v91\n"
      "v_cvt_pk_bf16_f32 v100, v104, v105\n"
      "v_cvt_pk_bf16_f32 v101, v106, v107\n"
      "v_exp_f32 v108, v92\n"
      "v_exp_f32 v109, v93\n"
      "v_mfma_f32_32x32x16_bf16 %[ob], %[v0], v[96:99], %[ob]\n"
      "ds_read_b128 %[v0], %[a16] offset:8704\n"
      "v_exp_f32 v110, v94\n"
      "v_exp_f32 v111, v95\n"
      "v_cvt_pk_bf16_f32 v102, v108, v109\n"
      "v_cvt_pk_bf16_f32 v103, v110, v111\n"
      "v_exp_f32 v104, v64\n"
      "v_exp_f32 v105, v65\n"
      "v_mfma_f32_32x32x16_bf16 %[ob], %[v1], v[100:103], %[ob]\n"
      "ds_read_b128 %[v1], %[a16] offset:9728\n"
      "v_exp_f32 v106, v66\n"
      "v_exp_f32 v107, v67\n"
      "v_cvt_pk_bf16_f32 v96, v104, v105\n"
      "v_cvt_pk_bf16_f32 v97, v106, v107\n"
      "v_exp_f32 v108, v68\n"
      "v_exp_f32 v109, v69\n"
      "v_mfma_f32_32x32x16_bf16 v[80:95], %[k1], %[q1b], 0\n"
      "v_exp_f32 v110, v70\n"
      "v_exp_f32 v111, v71\n"
      "v_cvt_pk_bf16_f32 v98, v108, v109\n"
      "v_cvt_pk_bf16_f32 v99, v110, v111\n"
      "v_exp_f32 v104, v72\n"
      "v_exp_f32 v105, v73\n"
      "v_mfma_f32_32x32x8_bf16 v[80:95], %[k2], %[q2b], v[80:95]\n"
      "ds_read_b128 %[k1], %[a16] offset:10752\n"
      "ds_read_b64 %[k2], %[a8] offset:11776\n"
      "v_exp_f32 v106, v74\n"
      "v_exp_f32 v107, v75\n"
      "v_cvt_pk_bf16_f32 v100, v104, v105\n"
      "v_cvt_pk_bf16_f32 v101, v106, v107\n"
      "v_exp_f32 v108, v76\n"
      "v_exp_f32 v109, v77\n"
      "s_waitcnt lgkmcnt(3)\n"
      "v_mfma_f32_32x32x16_bf16 %[oa], %[v0], v[96:99], %[oa]\n"
      "v_exp_f32 v110, v78\n"
      "v_exp_f32 v111, v79\n"
      "v_cvt_pk_bf16_f32 v102, v108, v109\n"
      "v_cvt_pk_bf16_f32 v103, v110, v111\n"
      "v_exp_f32 v104, v80\n"
      "v_exp_f32 v105, v81\n"
      "s_waitcnt lgkmcnt(2)\n"
      "v_mfma_f32_32x32x16_bf16 %[oa], %[v1], v[100:103], %[oa]\n"
      "v_exp_f32 v106, v82\n"
      "v_exp_f32 v107, v83\n"
      "v_cvt_pk_bf16_f32 v96, v104, v105\n"
      "v_cvt_pk_bf16_f32 v97, v106, v107\n"
      "v_exp_f32 v108, v84\n"
      "v_exp_f32 v109, v85\n"
      "s_waitcnt lgkmcnt(1)\n"
      "v_mfma_f32_32x32x16_bf16 v[64:79], %[k1], %[q1a], 0\n"
      "v_exp_f32 v110, v86\n"
      "v_exp_f32 v111, v87\n"
      "v_cvt_pk_bf16_f32 v98, v108, v109\n"
      "v_cvt_pk_bf16_f32 v99, v110, v111\n"
      "v_exp_f32 v104, v88\n"
      "v_exp_f32 v105, v89\n"
      "s_waitcnt lgkmcnt(0)\n"
      "v_mfma_f32_32x32x8_bf16 v[64:79], %[k2], %[q2a], v[64:79]\n"
      "v_exp_f32 v106, v90\n"
      "v_exp_f32 v107, v91\n"
      "v_cvt_pk_bf16_f32 v100, v104, v105\n"
      "v_cvt_pk_bf16_f32 v101, v106, v107\n"
      "v_exp_f32 v108, v92\n"
      "v_exp_f32 v109, v93\n"
      "v_mfma_f32_32x32x16_bf16 %[ob], %[v0], v[96:99], %[ob]\n"
      "ds_read_b128 %[v0], %[a16] offset:12288\n"
      "v_exp_f32 v110, v94\n"
      "v_exp_f32 v111, v95\n"
      "v_cvt_pk_bf16_f32 v102, v108, v109\n"
      "v_cvt_pk_bf16_f32 v103, v110, v111\n"
      "s_cmp_eq_u32 %[st], 7\n"
      "s_cbranch_scc1 2f\n"
      "s_add_u32 %[tmp], %[st], 2\n"
      "3:\n"
      "ds_read_b32 %[fv], %[fa]\n"
      "s_waitcnt lgkmcnt(0)\n"
      "v_readfirstlane_b32 %[t2], %[fv]\n"
      "s_cmp_ge_u32 %[t2], %[tmp]\n"
      "s_cbranch_scc1 2f\n"
      "s_sleep 1\n"
      "s_branch 3b\n"
      "2:\n"
      "v_exp_f32 v104, v64\n"
      "v_exp_f32 v105, v65\n"
      "v_mfma_f32_32x32x16_bf16 %[ob], %[v1], v[100:103], %[ob]\n"
      "ds_read_b128 %[v1], %[a16] offset:13312\n"
      "v_exp_f32 v106, v66\n"
      "v_exp_f32 v107, v67\n"
      "v_cvt_pk_bf16_f32 v96, v104, v105\n"
      "v_cvt_pk_bf16_f32 v97, v106, v107\n"
      "v_exp_f32 v108, v68\n"
      "v_exp_f32 v109, v69\n"
      "v_mfma_f32_32x32x16_bf16 v[80:95], %[k1], %[q1b], 0\n"
      "v_exp_f32 v110, v70\n"
      "v_exp_f32 v111, v71\n"
      "v_cvt_pk_bf16_f32 v98, v108, v109\n"
      "v_cvt_pk_bf16_f32 v99, v110, v111\n"
      "v_exp_f32 v104, v72\n"
      "v_exp_f32 v105, v73\n"
      "v_mfma_f32_32x32x8_bf16 v[80:95], %[k2], %[q2b], v[80:95]\n"
      "ds_read_b128 %[k1], %[n16]\n"
      "ds_read_b64 %[k2], %[n8] offset:1024\n"
      "v_exp_f32 v106, v74\n"
      "v_exp_f32 v107, v75\n"
      "v_cvt_pk_bf16_f32 v100, v104, v105\n"
      "v_cvt_pk_bf16_f32 v101, v106, v107\n"
      "v_exp_f32 v108, v76\n"
      "v_exp_f32 v109, v77\n"
      "s_waitcnt lgkmcnt(3)\n"
      "v_mfma_f32_32x32x16_bf16 %[oa], %[v0], v[96:99], %[oa]\n"
      "v_exp_f32 v110, v78\n"
      "v_exp_f32 v111, v79\n"
      "v_cvt_pk_bf16_f32 v102, v108, v109\n"
      "v_cvt_pk_bf16_f32 v103, v110, v111\n"
      "v_exp_f32 v104, v80\n"
      "v_exp_f32 v105, v81\n"
      "s_waitcnt lgkmcnt(2)\n"
      "v_mfma_f32_32x32x16_bf16 %[oa], %[v1], v[100:103], %[oa]\n"
      "v_exp_f32 v106, v82\n"
      "v_exp_f32 v107, v83\n"
      "v_cvt_pk_bf16_f32 v96, v104, v105\n"
      "v_cvt_pk_bf16_f32 v97, v106, v107\n"
      "v_exp_f32 v108, v84\n"
      "v_exp_f32 v109, v85\n"
      "s_waitcnt lgkmcnt(1)\n"
      "v_mfma_f32_32x32x16_bf16 v[64:79], %[k1], %[q1a], 0\n"
      "v_exp_f32 v110, v86\n"
      "v_exp_f32 v111, v87\n"
      "v_cvt_pk_bf16_f32 v98, v108, v109\n"
      "v_cvt_pk_bf16_f32 v99, v110, v111\n"
      "v_exp_f32 v104, v88\n"
      "v_exp_f32 v105, v89\n"
      "s_waitcnt lgkmcnt(0)\n"
      "v_mfma_f32_32x32x8_bf16 v[64:79], %[k2], %[q2a], v[64:79]\n"
      "v_exp_f32 v106, v90\n"
      "v_exp_f32 v107, v91\n"
      "v_cvt_pk_bf16_f32 v100, v104, v105\n"
      "v_cvt_pk_bf16_f32 v101, v106, v107\n"
      "v_exp_f32 v108, v92\n"
      "v_exp_f32 v109, v93\n"
      "v_mfma_f32_32x32x16_bf16 %[ob], %[v0], v[96:99], %[ob]\n"
      "ds_read_b128 %[v0], %[n16] offset:1536\n"
      "v_exp_f32 v110, v94\n"
      "v_exp_f32 v111, v95\n"
      "v_cvt_pk_bf16_f32 v102, v108, v109\n"
      "v_cvt_pk_bf16_f32 v103, v110, v111\n"
      "s_add_u32 %[st], %[st], 1\n"
      "v_mov_b32 %[fv], %[st]\n"
      "ds_write_b32 %[da], %[fv]\n"
      "s_mov_b32 %[tmp], %[b0]\n"
      "s_mov_b32 %[b0], %[b1]\n"
      "s_mov_b32 %[b1], %[b2]\n"
      "s_mov_b32 %[b2], %[tmp]\n"
      "s_cmp_lt_u32 %[st], 8\n"
      "s_cbranch_scc1 1b\n"
      "s_nop 1\n"
      "v_mfma_f32_32x32x16_bf16 %[ob], %[v1], v[100:103], %[ob]\n"
      "s_waitcnt lgkmcnt(0)\n"
      "s_nop 15\n"
      : [oa] "+v"(oa), [ob] "+v"(ob), [k1] "=&v"(k1), [k2] "=&v"(k2), [v0] "=&v"(v0), [v1] "+v"(v1),
        [a16] "=&v"(a16), [a8] "=&v"(a8), [n16] "=&v"(n16), [n8] "=&v"(n8),
        [b0] "+s"(b0), [b1] "+s"(b1), [b2] "+s"(b2), [st] "=&s"(st), [tmp] "=&s"(tmp), [t2] "=&s"(t2), [fv] "=&v"(fv)
      : [q1a] "v"(q1a), [q2a] "v"(q2a), [q1b] "v"(q1b), [q2b] "v"(q2b), [base16] "v"(base16), [base8] "v"(base8),
        [fa] "v"(fa), [da] "v"(da)
      : "memory", "scc", "v64", "v65", "v66", "v67", "v68", "v69", "v70", "v71", "v72", "v73", "v74", "v75", "v76", "v77", "v78", "v79", "v80", "v81", "v82", "v83", "v84", "v85", "v86", "v87", "v88", "v89", "v90", "v91", "v92", "v93", "v94", "v95", "v96", "v97", "v98", "v99", "v100", "v101", "v102", "v103", "v104", "v105", "v106", "v107", "v108", "v109", "v110", "v111");
  }
  float* ex = (float*)(lds + K2_EX + pr * 6144);
  if (kh == 0) {
#pragma unroll
    for (int r4 = 0; r4 < 3; ++r4) {
      *(f32x4*)(ex + r4 * 256 + lane * 4) = f32x4{oa[4 * r4], oa[4 * r4 + 1], oa[4 * r4 + 2], oa[4 * r4 + 3]};
      *(f32x4*)(ex + 768 + r4 * 256 + lane * 4) = f32x4{ob[4 * r4], ob[4 * r4 + 1], ob[4 * r4 + 2], ob[4 * r4 + 3]};
    }
    asm volatile("s_waitcnt lgkmcnt(0)" ::: "memory");
    fl[2 + pr] = 1u;
    return;
  }
  while (fl[2 + pr] == 0u) __builtin_amdgcn_s_sleep(1);
  asm volatile("" ::: "memory");
  const int h = lane & 31, hf = lane >> 5;
  const int s0 = sb + pr * 2;
  char* img = lds + K2_QI + pr * 2560;
#pragma unroll
  for (int a = 0; a < 2; ++a) {
    const f32x16& mine = a ? ob : oa;
    float o[12];
#pragma unroll
    for (int r4 = 0; r4 < 3; ++r4) {
      const f32x4 v = *(const f32x4*)(ex + a * 768 + r4 * 256 + lane * 4);
#pragma unroll
      for (int r = 0; r < 4; ++r) o[4 * r4 + r] = mine[4 * r4 + r] + v[r];
    }
    const int s = s0 + a;
    const float l = x32_sum(hf ? o[8] : 0.f);
    char* orow = img + a * 1280 + h * 40;
    const bool bad = !(l >= 8.2718061e-25f) || !(l <= 3.0e38f);
    if (__builtin_expect(__any(bad), 0)) {
      const char* qb = ws + WS_QP + (size_t)(s >> 5) * 80 * 512 + (s & 31) * 16;
      float q[20], oo[20];
#pragma unroll
      for (int d = 0; d < 20; ++d) { const int col = 20 * h + d; q[d] = bf2f(*(const __bf16*)(qb + (col >> 3) * 512 + (col & 7) * 2)); oo[d] = 0.f; }
      float m = -3.0e38f, ls = 0.f;
#pragma unroll 1
      for (int kk = 0; kk < S_; ++kk) {
        const char* tb = KVf + (size_t)(kk >> 5) * KV_TILE;
        const int kl = kk & 31;
        float sc = 0.f;
#pragma unroll
        for (int d = 0; d < 16; ++d) sc += q[d] * bf2f(*(const __bf16*)(tb + (kl + 32 * (d >> 3)) * 16 + (d & 7) * 2));
#pragma unroll
        for (int d = 16; d < 20; ++d) sc += q[d] * bf2f(*(const __bf16*)(tb + 1024 + kl * 8 + (d - 16) * 2));
        const float mn = fmaxf(m, sc), al = __builtin_amdgcn_exp2f(m - mn), p = __builtin_amdgcn_exp2f(sc - mn);
        const int sx = kl >> 4, k16 = kl & 15, vh = (k16 >> 2) & 1, jj = (k16 & 3) + 4 * (k16 >> 3);
        ls = ls * al + p;
#pragma unroll
        for (int d = 0; d < 20; ++d) oo[d] = oo[d] * al + p * bf2f(*(const __bf16*)(tb + 1536 + sx * 1024 + (d + 32 * vh) * 16 + jj * 2));
        m = mn;
      }
      const float inv = 1.f / ls;
      if (hf == 0) {
#pragma unroll
        for (int d4 = 0; d4 < 5; ++d4) *(bf16x4*)(orow + d4 * 8) = pack4(oo[4 * d4] * inv, oo[4 * d4 + 1] * inv, oo[4 * d4 + 2] * inv, oo[4 * d4 + 3] * inv);
      }
    } else {
      const float inv = __builtin_amdgcn_rcpf(l);
      *(bf16x4*)(orow + hf * 8) = pack4(o[0] * inv, o[1] * inv, o[2] * inv, o[3] * inv);
      *(bf16x4*)(orow + 16 + hf * 8) = pack4(o[4] * inv, o[5] * inv, o[6] * inv, o[7] * inv);
      if (hf == 0) *(bf16x4*)(orow + 32) = pack4(o[8] * inv, o[9] * inv, o[10] * inv, o[11] * inv);
    }
  }
  asm volatile("s_waitcnt lgkmcnt(0)" ::: "memory");
  {
    char* op = ws + WS_OB + (size_t)s0 * 1280 + lane * 16;
    st16(op, *(const u32x4*)(img + lane * 16));
    st16(op + 1024, *(const u32x4*)(img + 1024 + lane * 16));
    if (lane < 32) st16(op + 2048, *(const u32x4*)(img + 2048 + lane * 16));
  }
}

constexpr int K3_XB = 24576;

__global__ __launch_bounds__(512, 2) void k3_out(const __bf16* __restrict__ woA, const __bf16* __restrict__ woB,
                                                 char* __restrict__ ws, __bf16* __restrict__ out) {
  __shared__ __attribute__((aligned(16))) char lds[G_NB * G_STAGE];
  const int tid = threadIdx.x, lane = tid & 63, w = __builtin_amdgcn_readfirstlane(tid >> 6);
  const int T = (blockIdx.x & 7) * 28 + (blockIdx.x >> 3);
  if (T >= 220) return;
  const int rt = (T * 205) >> 11, ct = T - rt * 10;
  const unsigned lbase = (unsigned)(size_t)lds;
  const int l15 = lane & 15, g = lane >> 4;
  if (__builtin_expect(w >= 4, 1)) {
    const int lw = w - 4;
    const unsigned wv0 = (lane >> 3) * 1280 + ((((lane & 7) >> 1) ^ ((lane >> 4) & 1)) << 5) + ((lane & 1) << 4), wv1 = wv0 ^ 64;
    unsigned aoff[6], al[6];
#pragma unroll
    for (int ii = 0; ii < 6; ++ii) {
      int p = lw * 6 + ii + ct; p = p >= 24 ? p - 24 : p;
      const int row = 4 * p + (lane >> 4);
      int s = rt * 96 + row; s = s > S_ - 1 ? S_ - 1 : s;
      aoff[ii] = s * 1280 + (((lane & 15) ^ (row & 15)) << 4);
      al[ii] = p * 1024;
    }
    auto issue = [&](int st, int buf) {
      const int part = st >= 5, kk = st - 5 * part;
      const char* ab = ws + (part ? WS_OB : WS_OA) + kk * 256;
      const char* wp = (const char*)(part ? woB : woA) + (size_t)(kk * 128 + lw * 32) * 1280 + ct * 128;
      const unsigned lb = lbase + buf * G_STAGE;
      const unsigned wl = lb + K3_XB + lw * 4096;
      dma16(ab, aoff[0], lb + al[0]); dma16(wp, wv0, wl); dma16(ab, aoff[1], lb + al[1]);
      dma16(ab, aoff[2], lb + al[2]); dma16(wp + 8 * 1280, wv1, wl + 1024); dma16(ab, aoff[3], lb + al[3]);
      dma16(wp + 16 * 1280, wv0, wl + 2048); dma16(ab, aoff[4], lb + al[4]); dma16(ab, aoff[5], lb + al[5]);
      dma16(wp + 24 * 1280, wv1, wl + 3072);
    };
    issue(0, 0); issue(1, 1);
    WAIT_BAR(10);
    issue(2, 2);
    WAIT_BAR(10);
#pragma unroll 1
    for (int st = 0; st < 9; ++st) {
      if (st < 7) { issue(st + 3, (st + 3) & 3); WAIT_BAR(10); }
      else { WAIT_BAR(0); }
    }
  } else {
    const int wm = w >> 1, wn = w & 1;
    const int p4 = lane & 3, q4 = l15 >> 2;
    const int xo0 = (wm * 48 + l15) * 256 + ((g ^ l15) << 4);
    const int f = ((q4 >> 1) & 1) | ((g & 1) << 1);
    int wb[2];
#pragma unroll
    for (int j = 0; j < 2; ++j) wb[j] = K3_XB + (8 * g + q4) * 128 + 8 * p4 + 32 * ((2 * wn + j) ^ f);
    float gv[3];
#pragma unroll
    for (int mi = 0; mi < 3; ++mi) {
      int s = rt * 96 + wm * 48 + mi * 16 + l15; s = s > S_ - 1 ? S_ - 1 : s;
      const float gg = ((const float*)(ws + WS_G))[s];
      gv[mi] = gg;
    }
    f32x4 acc[3][2] = {}, accA[3][2];
    bf16x8 xf[4][3], wf[4][2];
    auto ldf = [&](int set, const char* sb, int ks) {
#pragma unroll
      for (int mi = 0; mi < 3; ++mi) xf[set][mi] = *(const bf16x8*)(sb + (xo0 ^ (ks * 64)) + mi * 4096);
#pragma unroll
      for (int ni = 0; ni < 2; ++ni) {
        const char* pw = sb + wb[ni] + ks * 32 * 128;
        wf[set][ni] = cat8(trread(pw), trread(pw + 4 * 128));
      }
    };
    auto mm = [&](int set) {
#pragma unroll
      for (int mi = 0; mi < 3; ++mi)
#pragma unroll
        for (int ni = 0; ni < 2; ++ni) acc[mi][ni] = mfma16(wf[set][ni], xf[set][mi], acc[mi][ni]);
#pragma unroll
      for (int i = 0; i < 6; ++i) { __builtin_amdgcn_sched_group_barrier(0x8, 1, 0); __builtin_amdgcn_sched_group_barrier(0x100, 1, 0); }
      __builtin_amdgcn_sched_group_barrier(0x100, 1, 0);
      __builtin_amdgcn_sched_barrier(0);
    };
    WAIT_BAR(10);
    ldf(0, lds, 0); ldf(1, lds, 1);
#pragma unroll
    for (int st = 0; st < 10; ++st) {
      const char* sb = lds + (st & 3) * G_STAGE;
      const char* sn = lds + ((st + 1) & 3) * G_STAGE;
      if (st == 5) {
#pragma unroll
        for (int mi = 0; mi < 3; ++mi)
#pragma unroll
          for (int ni = 0; ni < 2; ++ni) { accA[mi][ni] = acc[mi][ni] * gv[mi]; acc[mi][ni] = f32x4{0.f, 0.f, 0.f, 0.f}; }
      }
      ldf(2, sb, 2); mm(0);
      ldf(3, sb, 3); mm(1);
      if (st == 0) RAWBAR();
      if (st < 9) ldf(0, sn, 0);
      mm(2);
      if (st < 9) ldf(1, sn, 1);
      mm(3);
      if (st < 9) RAWBAR();
    }
#pragma unroll
    for (int mi = 0; mi < 3; ++mi) {
      const u32x4 v = pair_pack(accA[mi][0] + acc[mi][0] * (1.f - gv[mi]), accA[mi][1] + acc[mi][1] * (1.f - gv[mi]));
      *(u32x4*)(lds + (wm * 48 + mi * 16 + l15) * 144 + wn * 64 + (g & 1) * 32 + (g >> 1) * 16) = v;
    }
    BAR();
#pragma unroll
    for (int j = 0; j < 3; ++j) {
      const int row = w * 24 + j * 8 + (lane >> 3), s = rt * 96 + row;
      if (s < S_) st16((char*)out + (size_t)s * 1280 + ct * 128 + (lane & 7) * 16, *(const u32x4*)(lds + row * 144 + (lane & 7) * 16));
    }
  }
}

extern "C" void kernel_launch(void* const* d_in, const int* in_sizes, int n_in, void* d_out, int out_size, void* d_ws,
                              size_t ws_size, hipStream_t stream) {
  const __bf16* x = (const __bf16*)d_in[0];
  const __bf16* wqkv = (const __bf16*)d_in[1];
  const __bf16* woA = (const __bf16*)d_in[2];
  const __bf16* wq = (const __bf16*)d_in[4];
  const __bf16* wk = (const __bf16*)d_in[5];
  const __bf16* wv = (const __bf16*)d_in[6];
  const __bf16* woB = (const __bf16*)d_in[7];
  const __bf16* gw = (const __bf16*)d_in[8];
  char* ws = (char*)d_ws;
  k1_proj<<<256, 768, 0, stream>>>(x, wqkv, wq, wk, wv, gw, ws);
  k2_attn<<<256, 832, 0, stream>>>(ws, (const char*)woA, (const char*)woB);
  k3_out<<<224, 512, 0, stream>>>(woA, woB, ws, (__bf16*)d_out);
}
```

```cpp
#include <hip/hip_runtime.h>
#include <hip/hip_bf16.h>
#include <stdint.h>

#define DI __device__ __forceinline__
typedef __attribute__((ext_vector_type(8))) __bf16 bf16x8;
typedef __attribute__((ext_vector_type(4))) __bf16 bf16x4;
typedef __attribute__((ext_vector_type(4))) short s16x4;
typedef __attribute__((ext_vector_type(8))) short s16x8;
typedef __attribute__((ext_vector_type(16))) float f32x16;
typedef __attribute__((ext_vector_type(4))) float f32x4;
typedef __attribute__((ext_vector_type(4))) unsigned u32x4;
typedef __attribute__((ext_vector_type(2))) unsigned u32x2;
typedef __attribute__((address_space(3))) s16x4* lds_s16x4_ptr;

constexpr int S_ = 2048, D_ = 640;
constexpr float QSCALE = 0.22360679774997896f * 1.4426950408889634f;

constexpr size_t WS_QP = 0;
constexpr size_t WS_QKV = 2048 * 1280;
constexpr size_t WS_KV = WS_QKV + 2048 * 3840;
constexpr size_t WS_G = 13u << 20;
constexpr size_t WS_KMAX = (13u << 20) + 65536;
constexpr size_t WS_OA = 32u << 20;
constexpr size_t WS_OB = 36u << 20;
constexpr int KV_TILE = 3584;

DI f32x4 mfma16(bf16x8 a, bf16x8 b, f32x4 c) { return __builtin_amdgcn_mfma_f32_16x16x32_bf16(a, b, c, 0, 0, 0); }
DI s16x4 trread(const char* p) { return __builtin_amdgcn_ds_read_tr16_b64_v4i16((lds_s16x4_ptr)p); }
DI bf16x8 cat8(s16x4 lo, s16x4 hi) { return __builtin_bit_cast(bf16x8, __builtin_shufflevector(lo, hi, 0, 1, 2, 3, 4, 5, 6, 7)); }
DI bf16x4 pack4(float a, float b, float c, float d) { bf16x4 r; r[0] = (__bf16)a; r[1] = (__bf16)b; r[2] = (__bf16)c; r[3] = (__bf16)d; return r; }
DI float bf2f(__bf16 v) { return (float)v; }
DI float x16_max(float x) {
  auto s = __builtin_amdgcn_permlane16_swap(__float_as_uint(x), __float_as_uint(x), false, false);
  x = fmaxf(__uint_as_float(s[0]), __uint_as_float(s[1]));
  auto t = __builtin_amdgcn_permlane32_swap(__float_as_uint(x), __float_as_uint(x), false, false);
  return fmaxf(__uint_as_float(t[0]), __uint_as_float(t[1]));
}
DI float x16_sum(float x) {
  auto s = __builtin_amdgcn_permlane16_swap(__float_as_uint(x), __float_as_uint(x), false, false);
  x = __uint_as_float(s[0]) + __uint_as_float(s[1]);
  auto t = __builtin_amdgcn_permlane32_swap(__float_as_uint(x), __float_as_uint(x), false, false);
  return __uint_as_float(t[0]) + __uint_as_float(t[1]);
}
DI float x32_sum(float x) {
  auto t = __builtin_amdgcn_permlane32_swap(__float_as_uint(x), __float_as_uint(x), false, false);
  return __uint_as_float(t[0]) + __uint_as_float(t[1]);
}
template <int CTRL> DI float dppf(float x) { return __builtin_bit_cast(float, __builtin_amdgcn_mov_dpp(__builtin_bit_cast(int, x), CTRL, 0xf, 0xf, true)); }
DI float row16_max(float x) {
  x = fmaxf(x, dppf<0xB1>(x)); x = fmaxf(x, dppf<0x4E>(x)); x = fmaxf(x, dppf<0x141>(x)); x = fmaxf(x, dppf<0x140>(x));
  return x;
}

DI u32x4 pair_pack(const f32x4& a, const f32x4& b) {
  u32x2 pa = __builtin_bit_cast(u32x2, pack4(a[0], a[1], a[2], a[3])), pb = __builtin_bit_cast(u32x2, pack4(b[0], b[1], b[2], b[3]));
  auto s0 = __builtin_amdgcn_permlane16_swap(pa[0], pb[0], false, false);
  auto s1 = __builtin_amdgcn_permlane16_swap(pa[1], pb[1], false, false);
  return u32x4{s0[0], s1[0], s0[1], s1[1]};
}
DI void st16(void* p, u32x4 v) { asm volatile("global_store_dwordx4 %0, %1, off sc1\n\ts_nop 1" :: "v"(p), "v"(v) : "memory"); }
DI void st8(void* p, u32x2 v) { asm volatile("global_store_dwordx2 %0, %1, off sc1" :: "v"(p), "v"(v) : "memory"); }
DI const void* uni_ptr(const void* p) {
  const unsigned long long a = (unsigned long long)p;
  const unsigned lo = __builtin_amdgcn_readfirstlane((unsigned)a), hi = __builtin_amdgcn_readfirstlane((unsigned)(a >> 32));
  return (const void*)(((unsigned long long)hi << 32) | lo);
}
DI void dma16(const void* sbase, unsigned voff, unsigned lds_dst) {
  sbase = uni_ptr(sbase); lds_dst = __builtin_amdgcn_readfirstlane(lds_dst);
  unsigned keep;
  asm volatile("s_mov_b32 %0, m0\n\ts_mov_b32 m0, %3\n\ts_nop 2\n\tglobal_load_lds_dwordx4 %1, %2\n\ts_mov_b32 m0, %0"
               : "=&s"(keep) : "v"(voff), "s"(sbase), "s"(lds_dst) : "memory");
}
DI void dma4(const void* sbase, unsigned voff, unsigned lds_dst) {
  sbase = uni_ptr(sbase); lds_dst = __builtin_amdgcn_readfirstlane(lds_dst);
  unsigned keep;
  asm volatile("s_mov_b32 %0, m0\n\ts_mov_b32 m0, %3\n\ts_nop 2\n\tglobal_load_lds_dword %1, %2\n\ts_mov_b32 m0, %0"
               : "=&s"(keep) : "v"(voff), "s"(sbase), "s"(lds_dst) : "memory");
}
#define WAIT_BAR(n) asm volatile("s_waitcnt vmcnt(" #n ") lgkmcnt(0)\n\ts_barrier" ::: "memory")
#define BAR() asm volatile("s_waitcnt lgkmcnt(0)\n\ts_barrier" ::: "memory")
#define RAWBAR() asm volatile("s_barrier" ::: "memory")

constexpr int G_STAGE = 40960, G_NB = 4;
constexpr int K1_XB = 16384;

DI void k1_kv_block(char* lds, int kvb, int w, int lane, const __bf16* x, const __bf16* wk, const __bf16* wv,
                    const __bf16* gw, char* KVf, float* gout, float* kmax2) {
  const unsigned lbase = (unsigned)(size_t)lds;
  const int tid = w * 64 + lane, l15 = lane & 15, g = lane >> 4;
  constexpr int WK = 81920, WV = WK + 25600, GW = WV + 25600;
  {
    const char* xb = (const char*)x + (size_t)kvb * 64 * 1280;
#pragma unroll 2
    for (int p = w; p < 80; p += 12) {
      const int q = p * 64 + lane, row = q / 80, cp = q - row * 80;
      dma16(xb, row * 1280 + (((cp & ~7) | ((cp ^ row) & 7)) << 4), lbase + p * 1024);
    }
    for (int p = w; p < 50; p += 12) {
      const char* src = (p < 25) ? (const char*)wk + p * 1024 : (const char*)wv + (p - 25) * 1024;
      dma16(src, lane * 16, lbase + WK + p * 1024);
    }
    for (int k = tid; k < 640; k += 768) {
      bf16x4 v; v[0] = gw[k]; v[1] = (__bf16)0.f; v[2] = (__bf16)0.f; v[3] = (__bf16)0.f;
      *(bf16x4*)(lds + GW + k * 40) = v;
    }
  }
  WAIT_BAR(0);
  f32x4 acc[3] = {};
  const int row = (w & 3) * 16 + l15;
  if (w < 4) {
    const int p4 = lane & 3, q4 = l15 >> 2;
    int wa[3];
#pragma unroll
    for (int ni = 0; ni < 3; ++ni) {
      const int cg = 4 * ni + p4;
      wa[ni] = (cg < 5 ? WK + cg * 8 : (cg < 10 ? WV + (cg - 5) * 8 : GW)) + (8 * g + q4) * 40;
    }
    const int xo = row * 1280 + ((g ^ (row & 7)) << 4);
#pragma unroll
    for (int ks = 0; ks < 20; ++ks) {
      const bf16x8 xf = *(const bf16x8*)(lds + (xo ^ ((ks & 1) * 64)) + (ks >> 1) * 128);
#pragma unroll
      for (int ni = 0; ni < 3; ++ni) {
        const char* pw = lds + wa[ni] + ks * 32 * 40;
        acc[ni] = mfma16(cat8(trread(pw), trread(pw + 160)), xf, acc[ni]);
      }
    }
  }
  BAR();
  if (w < 4) {
#pragma unroll
    for (int ni = 0; ni < 3; ++ni) {
      const int cg = 4 * ni + g;
      const bf16x4 v = pack4(acc[ni][0], acc[ni][1], acc[ni][2], acc[ni][3]);
      if (cg < 5) *(bf16x4*)(lds + row * 96 + cg * 8) = v;
      else if (cg < 10) *(bf16x4*)(lds + row * 96 + 48 + (cg - 5) * 8) = v;
      else if (cg == 10) gout[kvb * 64 + row] = 1.f / (1.f + __expf(-acc[ni][0]));
    }
  }
  BAR();
  if (w >= 2) return;
  const char* T = lds + w * 32 * 96;
  const int t = kvb * 2 + w;
  char* dst = KVf + (size_t)t * KV_TILE;
  const int key = lane & 31, hf = lane >> 5;
  const u32x4 k1 = *(const u32x4*)(T + key * 96 + hf * 16);
  u32x2 k2 = *(const u32x2*)(T + key * 96 + 32);
  {
    const bf16x8 a = __builtin_bit_cast(bf16x8, k1); const bf16x4 b = __builtin_bit_cast(bf16x4, k2);
    float n = 0.f;
#pragma unroll
    for (int j = 0; j < 8; ++j) n += bf2f(a[j]) * bf2f(a[j]);
    float n3 = 0.f;
#pragma unroll
    for (int j = 0; j < 4; ++j) n3 += bf2f(b[j]) * bf2f(b[j]);
    n = x32_sum(n) + n3;
    n = x16_max(row16_max(n));
    if (lane == 0) kmax2[t] = n;
  }
  st16(dst + lane * 16, k1);
  if (hf) { k2[0] = 0x3F80u; k2[1] = 0u; }
  st8(dst + 1024 + lane * 8, k2);
#pragma unroll
  for (int s = 0; s < 2; ++s) {
    s16x8 v;
#pragma unroll
    for (int j = 0; j < 8; ++j) {
      const int kk = 16 * s + 8 * (j >> 2) + 4 * hf + (j & 3);
      const short e = *(const short*)(T + kk * 96 + 48 + (key < 20 ? key : 0) * 2);
      v[j] = key < 20 ? e : (key == 20 ? (short)0x3F80 : (short)0);
    }
    st16(dst + 1536 + s * 1024 + lane * 16, __builtin_bit_cast(u32x4, v));
  }
}

__global__ __launch_bounds__(768, 3) void k1_proj(const __bf16* __restrict__ x, const __bf16* __restrict__ wqkv,
                                                  const __bf16* __restrict__ wq, const __bf16* __restrict__ wk,
                                                  const __bf16* __restrict__ wv, const __bf16* __restrict__ gw,
                                                  char* __restrict__ ws) {
  __shared__ __attribute__((aligned(16))) char lds[G_NB * G_STAGE];
  const int tid = threadIdx.x, lane = tid & 63, w = __builtin_amdgcn_readfirstlane(tid >> 6);
  const int b = blockIdx.x, xcd = b & 7, idx = b >> 3;
  if (__builtin_expect(idx >= 28, 0)) {
    k1_kv_block(lds, xcd * 4 + (idx - 28), w, lane, x, wk, wv, gw, ws + WS_KV, (float*)(ws + WS_G), (float*)(ws + WS_KMAX));
    return;
  }
  const int rl = (idx * 37) >> 8, cl7 = idx - rl * 7;
  const int rt = 4 * (xcd >> 1) + rl, ct = 7 * (xcd & 1) + cl7;
  const unsigned lbase = (unsigned)(size_t)lds;
  const int l15 = lane & 15, g = lane >> 4;
  if (__builtin_expect(w >= 8, 1)) {
    const int lw = w - 8;
    const int rX = cl7, rW = (0x5320 >> (4 * rl)) & 15;
    const char* xb0 = (const char*)x + (size_t)rt * 128 * 1280;
    const unsigned xe = (lane >> 3) * 1280 + (((lane & 7) ^ (lane >> 4)) << 4);
    const unsigned xv0 = xe ^ ((rX & 1) << 6), xv1 = xv0 ^ 64;
    const bool isq = ct >= 10;
    const unsigned wstride = isq ? 1280u : 3840u;
    const char* wb0 = (isq ? (const char*)wq + (ct - 10) * 384 : (const char*)wqkv + ct * 384);
    const unsigned wv0 = (lane >> 3) * wstride + ((((lane & 7) >> 1) ^ ((lane >> 4) & 1)) << 5) + ((lane & 1) << 4);
    auto issue = [&](int st, int buf) {
      const char* xb = xb0 + st * 128;
      const char* wp = wb0 + (size_t)st * 64 * wstride;
      const unsigned lb = lbase + buf * G_STAGE;
      auto xp = [&](int ii) { const int p = (lw * 4 + ii + rX) & 15; dma16(xb + p * 8 * 1280, (ii & 1) ? xv1 : xv0, lb + p * 1024); };
      auto wpc = [&](int ii) {
        int p = lw * 6 + ii + rW; p = p >= 24 ? p - 24 : p;
        const int ko = (p * 43) >> 7, th = p - ko * 3;
        dma16(wp + (size_t)ko * 8 * wstride + (ct == 13 ? 0 : th * 128), wv0 ^ ((ko & 1) << 6), lb + K1_XB + p * 1024);
      };
      xp(0); wpc(0); wpc(1); xp(1); wpc(2); xp(2); wpc(3); wpc(4); xp(3); wpc(5);
    };
    issue(0, 0); issue(1, 1);
    WAIT_BAR(10);
    issue(2, 2);
    WAIT_BAR(10);
#pragma unroll 1
    for (int st = 0; st < 9; ++st) {
      if (st < 7) { issue(st + 3, (st + 3) & 3); WAIT_BAR(10); }
      else { WAIT_BAR(0); }
    }
  } else {
    const int wm = w >> 2, wn = w & 3;
    if (ct == 13 && wn >= 2) {
#pragma unroll 1
      for (int i = 0; i < 11; ++i) RAWBAR();
      return;
    }
    const int p4 = lane & 3, q4 = l15 >> 2;
    const int xo0 = (wm * 64 + l15) * 128 + ((g ^ ((l15 >> 1) & 7)) << 4);
    int wo[3];
    {
      const int f = ((q4 >> 1) & 1) | ((g & 1) << 1);
#pragma unroll
      for (int ni = 0; ni < 3; ++ni) { const int bb = 3 * wn + ni; wo[ni] = K1_XB + (g * 3 + (bb >> 2)) * 1024 + q4 * 128 + (((bb & 3) ^ f) << 5) + 8 * p4; }
    }
    f32x4 acc[4][3] = {};
    bf16x8 xa[4], wa[3], xb[4], wc[3];
    auto ldf = [&](bf16x8 (&xf)[4], bf16x8 (&wf)[3], const char* sb, int ks) {
#pragma unroll
      for (int mi = 0; mi < 4; ++mi) xf[mi] = *(const bf16x8*)(sb + (xo0 ^ (ks * 64)) + mi * 2048);
#pragma unroll
      for (int ni = 0; ni < 3; ++ni) {
        const char* pw = sb + wo[ni] + ks * 12 * 1024;
        wf[ni] = cat8(trread(pw), trread(pw + 512));
      }
    };
    auto mm = [&](const bf16x8 (&xf)[4], const bf16x8 (&wf)[3]) {
#pragma unroll
      for (int mi = 0; mi < 4; ++mi)
#pragma unroll
        for (int ni = 0; ni < 3; ++ni) acc[mi][ni] = mfma16(wf[ni], xf[mi], acc[mi][ni]);
#pragma unroll
      for (int i = 0; i < 10; ++i) { __builtin_amdgcn_sched_group_barrier(0x8, 1, 0); __builtin_amdgcn_sched_group_barrier(0x100, 1, 0); }
      __builtin_amdgcn_sched_group_barrier(0x8, 2, 0);
      __builtin_amdgcn_sched_barrier(0);
    };
    RAWBAR();
    ldf(xa, wa, lds, 0);
#pragma unroll
    for (int st = 0; st < 10; ++st) {
      const char* sb = lds + (st & 3) * G_STAGE;
      const char* sn = lds + ((st + 1) & 3) * G_STAGE;
      ldf(xb, wc, sb, 1); mm(xa, wa);
      if (st == 0) RAWBAR();
      if (st < 9) ldf(xa, wa, sn, 0);
      mm(xb, wc);
      if (st < 9) RAWBAR();
    }
    char* dst; int no, oc0;
    if (ct >= 10) {
      dst = ws + WS_QP; no = 80; oc0 = (ct - 10) * 24;
#pragma unroll
      for (int mi = 0; mi < 4; ++mi)
#pragma unroll
        for (int ni = 0; ni < 3; ++ni) acc[mi][ni] *= QSCALE;
    } else { dst = ws + WS_QKV; no = 240; oc0 = ct * 24; }
    oc0 += wn * 6;
    dst += (size_t)((rt * 4 + wm * 2) * no) * 512 + l15 * 16;
    const int ocp = oc0 + (g & 1) * 2 + (g >> 1), ocs = oc0 + 4 + (g >> 1);
#pragma unroll
    for (int mi = 0; mi < 4; ++mi) {
      char* d = dst + (size_t)(mi >> 1) * no * 512 + (mi & 1) * 256;
      const u32x4 v = pair_pack(acc[mi][0], acc[mi][1]);
      if (ocp < no) st16(d + (size_t)ocp * 512, v);
      if (ocs < no) st8(d + (size_t)ocs * 512 + (g & 1) * 8, __builtin_bit_cast(u32x2, pack4(acc[mi][2][0], acc[mi][2][1], acc[mi][2][2], acc[mi][2][3])));
    }
  }
}

constexpr int K2_STAGE = 8 * KV_TILE;
constexpr int K2_RA = 3 * K2_STAGE;
constexpr int K2_QI = K2_RA + 38 * 1024;
constexpr int K2_FL = K2_QI + 10400;
constexpr int K2_SCR = K2_FL + 2304;

DI float dot2bf(unsigned a, float c) {
  typedef __attribute__((ext_vector_type(2))) __bf16 bf16x2;
  return __builtin_amdgcn_fdot2_f32_bf16(__builtin_bit_cast(bf16x2, a), __builtin_bit_cast(bf16x2, a), c, false);
}
DI void prep_q(const char* qi, int lane, float km, bf16x8& q1, s16x4& q2) {
  const int h = lane & 31, hf = lane >> 5;
  auto qa = [&](int col) { return *(const u32x2*)(qi + (col >> 3) * 128 + (col >> 6) * 16 + (col & 7) * 2); };
  const u32x2 lo = qa(20 * h + 8 * hf), hi = qa(20 * h + 8 * hf + 4), tl = qa(20 * h + 16);
  q1 = __builtin_bit_cast(bf16x8, u32x4{lo[0], lo[1], hi[0], hi[1]});
  const float n2 = x32_sum(dot2bf(lo[0], dot2bf(lo[1], dot2bf(hi[0], dot2bf(hi[1], 0.f))))) + dot2bf(tl[0], dot2bf(tl[1], 0.f));
  const float off = __builtin_amdgcn_sqrtf(n2 * km) * 1.01f - 80.f;
  unsigned ob = __float_as_uint(off);
  ob = (off > 0.f) ? ((ob + 0xFFFFu) & 0xFFFF0000u) : (ob & 0xFFFF0000u);
  u32x2 t2 = tl;
  if (hf) { t2[0] = (ob >> 16) ^ 0x8000u; t2[1] = 0u; }
  q2 = __builtin_bit_cast(s16x4, t2);
}

__global__ __launch_bounds__(576, 3) void k2_attn(char* __restrict__ ws, const char* __restrict__ woA, const char* __restrict__ woB) {
  __shared__ __attribute__((aligned(16))) char lds[K2_SCR + 256];
  const int tid = threadIdx.x, lane = tid & 63, w = __builtin_amdgcn_readfirstlane(tid >> 6);
  const unsigned lbase = (unsigned)(size_t)lds;
  const char* KVf = ws + WS_KV;
  volatile __attribute__((address_space(3))) unsigned* fl = (volatile __attribute__((address_space(3))) unsigned*)(lds + K2_FL);
  const int pr = w & 3;
  const int sb = blockIdx.x * 8;
  const int hp = blockIdx.x & 15, art = blockIdx.x >> 4;
  {
    const char* qp = ws + WS_QP + (size_t)(sb >> 5) * 80 * 512 + (sb & 31) * 16;
    const unsigned qv = (lane >> 3) * 512 + (lane & 7) * 16;
#pragma unroll
    for (int i = 0; i < 5; ++i) {
      const int q = w + 9 * i;
      if (q < 10) dma16(qp + q * 4096, qv, lbase + K2_QI + q * 1040);
      else if (q < 38) dma16(KVf + (q - 10) * 1024, lane * 16, lbase + (q - 10) * 1024);
    }
    if (w >= 4) for (int i = tid - 256; i < 576; i += 320) fl[i] = i == 0 ? 1u : 0u;
  }
  float km = ((const float*)(ws + WS_KMAX))[lane];
  km = x16_max(row16_max(km));
  asm volatile("" : "+v"(km));
  WAIT_BAR(0);
  if (w == 4) {
    __builtin_amdgcn_s_setprio(3);
    const unsigned dv = lane * 16;
#pragma unroll 1
    for (int st = 1; st < 8; ++st) {
      if (st >= 3) {
        while (true) {
          const unsigned d = fl[64 + 64 * (lane & 3)];
          if (__all(d >= (unsigned)(st - 2))) break;
          __builtin_amdgcn_s_sleep(2);
        }
        asm volatile("" ::: "memory");
      }
      const char* src = KVf + st * K2_STAGE;
      const unsigned ld = lbase + (st % 3) * K2_STAGE;
#pragma unroll 4
      for (int k = 0; k < 28; ++k) dma16(src + k * 1024, dv, ld + k * 1024);
      asm volatile("s_waitcnt vmcnt(0)" ::: "memory");
      fl[0] = st + 1;
      if (st == 4) {
        const int grp = blockIdx.x >> 3;
#pragma unroll
        for (int k = 0; k < 7; ++k) {
          int ln = k * 64 + lane; ln = ln > 399 ? 399 : ln;
          dma4(grp >= 16 ? woB : woA, ((grp & 15) * 400 + ln) * 128, lbase + K2_SCR);
        }
      }
    }
    return;
  }
  if (w > 4) {
    {
      const char* rap = ws + WS_QKV + ((long)art * 4 - 1) * 240 * 512 + hp * 5 * 512;
      unsigned rp[3];
#pragma unroll
      for (int t = 0; t < 3; ++t) { const int r = 16 + 64 * t + lane; rp[t] = (r >> 5) * 240 * 512 + (r & 31) * 16; }
#pragma unroll 1
      for (int co = w - 5; co < 15; co += 4) {
        const int seg = (co * 13) >> 6;
        const char* src = rap + (seg * 75 + co) * 512;
        const unsigned ld = lbase + K2_RA + co * 2560;
        dma16(src, rp[0], ld); dma16(src, rp[1], ld + 1024);
        if (lane < 32) dma16(src, rp[2], ld + 2048);
      }
      asm volatile("s_waitcnt vmcnt(0)" ::: "memory");
      if (lane == 0) __hip_atomic_fetch_add((__attribute__((address_space(3))) unsigned*)(lds + K2_FL + 4), 1u, __ATOMIC_RELAXED, __HIP_MEMORY_SCOPE_WORKGROUP);
    }
    while (fl[1] < 4u) __builtin_amdgcn_s_sleep(8);
    asm volatile("" ::: "memory");
    const int l15 = lane & 15, g = lane >> 4, p4 = lane & 3, q4 = l15 >> 2;
    const int hh = w & 1, par = ((w - 5) >> 1) & 1;
    constexpr int TS = 4 * 128;
    const char* RA = lds + K2_RA;
    auto ea = [&](int u, int cc) { const int i = 2 * u + par; return RA + ((cc >> 3) * 20 + (i >> 3)) * 128 + (i & 7) * 16 + (cc & 7) * 2; };
    const int gk = g < 3 ? 8 * g : 0;
    const char* klo = ea(l15, 40 + 20 * hh + gk);
    const char* khi = ea(l15, 40 + 20 * hh + gk + 4);
    const char* vp0 = ea(4 * g + q4, 80 + 20 * hh + 4 * p4);
    const char* vp1 = ea(4 * g + q4, 80 + 20 * hh + 16 + (hh == 1 && p4 > 0 ? 0 : 4 * p4));
    const char* qlo = ea(8 + l15, 20 * hh + gk);
    const char* qhi = ea(8 + l15, 20 * hh + gk + 4);
    bool m0[4], m1[4];
#pragma unroll
    for (int r = 0; r < 4; ++r) { const int j = 4 * g + r; m0[r] = (j >= l15 + 1) && (j <= l15 + 8); m1[r] = (j <= l15 - 8); }
          const f32x4 z4 = {0.f, 0.f, 0.f, 0.f};
    const s16x4 zz = {0, 0, 0, 0};
    const float ninf = -__builtin_inff();
    bf16x8 kc = cat8(*(const s16x4*)klo, *(const s16x4*)khi);
    s16x4 vc0 = trread(vp0), vc1 = trread(vp1);
#pragma unroll
    for (int y = 0; y < 4; ++y) {
      const bf16x8 kn = cat8(*(const s16x4*)(klo + (y + 1) * TS), *(const s16x4*)(khi + (y + 1) * TS));
      const s16x4 vn0 = trread(vp0 + (y + 1) * TS), vn1 = trread(vp1 + (y + 1) * TS);
      s16x4 a = *(const s16x4*)(qlo + y * TS), b = *(const s16x4*)(qhi + y * TS);
      if (g == 3) a = zz;
      if (g >= 2) b = zz;
      const bf16x8 qf = cat8(a, b);
      f32x4 s0v = mfma16(kc, qf, z4), s1v = mfma16(kn, qf, z4);
#pragma unroll
      for (int r = 0; r < 4; ++r) {
        const bool a0 = (y == 0) ? (m0[r] && !(art == 0 && 4 * g + r < 8)) : m0[r];
        s0v[r] = a0 ? s0v[r] : ninf;
        s1v[r] = m1[r] ? s1v[r] : ninf;
      }
      float mx = fmaxf(__builtin_fmaxf(__builtin_fmaxf(s0v[0], s0v[1]), s0v[2]), __builtin_fmaxf(__builtin_fmaxf(s0v[3], s1v[0]), s1v[1]));
      mx = fmaxf(mx, fmaxf(s1v[2], s1v[3]));
      mx = x16_max(mx);
      const float mc = -mx * QSCALE;
      float p0[4], p1[4];
#pragma unroll
      for (int r = 0; r < 4; ++r) {
        p0[r] = __builtin_amdgcn_exp2f(__builtin_fmaf(s0v[r], QSCALE, mc));
        p1[r] = __builtin_amdgcn_exp2f(__builtin_fmaf(s1v[r], QSCALE, mc));
      }
      const float ls = ((p0[0] + p0[1]) + (p0[2] + p0[3])) + ((p1[0] + p1[1]) + (p1[2] + p1[3]));
      const float inv = __builtin_amdgcn_rcpf(x16_sum(ls));
      bf16x8 pf;
#pragma unroll
      for (int r = 0; r < 4; ++r) { pf[r] = (__bf16)p0[r]; pf[4 + r] = (__bf16)p1[r]; }
      const f32x4 o0 = mfma16(cat8(vc0, vn0), pf, z4);
      const f32x4 o1 = mfma16(cat8(vc1, vn1), pf, z4);
      const int s = art * 128 + 32 * y + 2 * l15 + par;
      char* orow = ws + WS_OA + (size_t)s * 1280 + (2 * hp + hh) * 40;
      st8(orow + g * 8, __builtin_bit_cast(u32x2, pack4(o0[0] * inv, o0[1] * inv, o0[2] * inv, o0[3] * inv)));
      if (g == 0) st8(orow + 32, __builtin_bit_cast(u32x2, pack4(o1[0] * inv, o1[1] * inv, o1[2] * inv, o1[3] * inv)));
      kc = kn; vc0 = vn0; vc1 = vn1;
    }
    return;
  }
  bf16x8 q1a, q1b; s16x4 q2a, q2b;
  prep_q(lds + K2_QI + (pr * 2) * 16, lane, km, q1a, q2a);
  prep_q(lds + K2_QI + (pr * 2 + 1) * 16, lane, km, q1b, q2b);
  f32x16 oa = {}, ob = {};
  {
    u32x4 k1, v0, v0b, v1 = {0u, 0u, 0u, 0u}; u32x2 k2;
    unsigned a16, a8, n16, n8;
    const unsigned base16 = lbase + lane * 16, base8 = lbase + lane * 8;
    unsigned b0 = 0, b1 = K2_STAGE, b2 = 2 * K2_STAGE, st, tmp, t2, fv;
    const unsigned fa = lbase + K2_FL, da = lbase + K2_FL + 256 + w * 256 + lane * 4;
    asm volatile(
      "v_add_u32 %[a16], %[b0], %[base16]\n"
      "v_add_u32 %[a8], %[b0], %[base8]\n"
      "ds_read_b128 %[k1], %[a16]\n"
      "ds_read_b64 %[k2], %[a8] offset:1024\n"
      "ds_read_b128 %[v0], %[a16] offset:1536\n"
      "v_mov_b32 v100, 0\n"
      "v_mov_b32 v101, 0\n"
      "v_mov_b32 v102, 0\n"
      "v_mov_b32 v103, 0\n"
      "s_waitcnt lgkmcnt(0)\n"
      "v_mfma_f32_32x32x16_bf16 v[64:79], %[k1], %[q1a], 0\n"
      "v_mfma_f32_32x32x8_bf16 v[64:79], %[k2], %[q2a], v[64:79]\n"
      "s_mov_b32 %[st], 0\n"
      "s_nop 7\n"
      "s_nop 7\n"
      ".p2align 6\n"
      "s_nop 0\n"
      "1:\n"
      "v_add_u32 %[a16], %[b0], %[base16]\n"
      "v_add_u32 %[a8], %[b0], %[base8]\n"
      "v_add_u32 %[n16], %[b1], %[base16]\n"
      "v_add_u32 %[n8], %[b1], %[base8]\n"
      "v_exp_f32 v104, v64\n"
      "v_exp_f32 v105, v65\n"
      "v_mfma_f32_32x32x16_bf16 %[ob], %[v1], v[100:103], %[ob]\n"
      "ds_read_b128 %[v1], %[a16] offset:2560\n"
      "ds_read_b128 %[v0b], %[a16] offset:5120\n"
      "v_exp_f32 v106, v66\n"
      "v_exp_f32 v107, v67\n"
      "v_cvt_pk_bf16_f32 v96, v104, v105\n"
      "v_cvt_pk_bf16_f32 v97, v106, v107\n"
      "v_exp_f32 v108, v68\n"
      "v_exp_f32 v109, v69\n"
      "v_mfma_f32_32x32x16_bf16 v[80:95], %[k1], %[q1b], 0\n"
      "ds_read_b128 %[k1], %[a16] offset:3584\n"
      "v_exp_f32 v110, v70\n"
      "v_exp_f32 v111, v71\n"
      "v_cvt_pk_bf16_f32 v98, v108, v109\n"
      "v_cvt_pk_bf16_f32 v99, v110, v111\n"
      "v_exp_f32 v104, v72\n"
      "v_exp_f32 v105, v73\n"
      "v_mfma_f32_32x32x8_bf16 v[80:95], %[k2], %[q2b], v[80:95]\n"
      "ds_read_b64 %[k2], %[a8] offset:4608\n"
      "v_exp_f32 v106, v74\n"
      "v_exp_f32 v107, v75\n"
      "v_cvt_pk_bf16_f32 v100, v104, v105\n"
      "v_cvt_pk_bf16_f32 v101, v106, v107\n"
      "v_exp_f32 v108, v76\n"
      "v_exp_f32 v109, v77\n"
      "v_mfma_f32_32x32x16_bf16 %[oa], %[v0], v[96:99], %[oa]\n"
      "v_exp_f32 v110, v78\n"
      "v_exp_f32 v111, v79\n"
      "v_cvt_pk_bf16_f32 v102, v108, v109\n"
      "v_cvt_pk_bf16_f32 v103, v110, v111\n"
      "v_exp_f32 v104, v80\n"
      "v_exp_f32 v105, v81\n"
      "s_waitcnt lgkmcnt(0)\n"
      "v_mfma_f32_32x32x16_bf16 %[oa], %[v1], v[100:103], %[oa]\n"
      "v_exp_f32 v106, v82\n"
      "v_exp_f32 v107, v83\n"
      "v_cvt_pk_bf16_f32 v96, v104, v105\n"
      "v_cvt_pk_bf16_f32 v97, v106, v107\n"
      "v_exp_f32 v108, v84\n"
      "v_exp_f32 v109, v85\n"
      "v_mfma_f32_32x32x16_bf16 v[64:79], %[k1], %[q1a], 0\n"
      "v_exp_f32 v110, v86\n"
      "v_exp_f32 v111, v87\n"
      "v_cvt_pk_bf16_f32 v98, v108, v109\n"
      "v_cvt_pk_bf16_f32 v99, v110, v111\n"
      "v_exp_f32 v104, v88\n"
      "v_exp_f32 v105, v89\n"
      "v_mfma_f32_32x32x8_bf16 v[64:79], %[k2], %[q2a], v[64:79]\n"
      "v_exp_f32 v106, v90\n"
      "v_exp_f32 v107, v91\n"
      "v_cvt_pk_bf16_f32 v100, v104, v105\n"
      "v_cvt_pk_bf16_f32 v101, v106, v107\n"
      "v_exp_f32 v108, v92\n"
      "v_exp_f32 v109, v93\n"
      "v_mfma_f32_32x32x16_bf16 %[ob], %[v0], v[96:99], %[ob]\n"
      "v_exp_f32 v110, v94\n"
      "v_exp_f32 v111, v95\n"
      "v_cvt_pk_bf16_f32 v102, v108, v109\n"
      "v_cvt_pk_bf16_f32 v103, v110, v111\n"
      "v_exp_f32 v104, v64\n"
      "v_exp_f32 v105, v65\n"
      "v_mfma_f32_32x32x16_bf16 %[ob], %[v1], v[100:103], %[ob]\n"
      "ds_read_b128 %[v1], %[a16] offset:6144\n"
      "ds_read_b128 %[v0], %[a16] offset:8704\n"
      "v_exp_f32 v106, v66\n"
      "v_exp_f32 v107, v67\n"
      "v_cvt_pk_bf16_f32 v96, v104, v105\n"
      "v_cvt_pk_bf16_f32 v97, v106, v107\n"
      "v_exp_f32 v108, v68\n"
      "v_exp_f32 v109, v69\n"
      "v_mfma_f32_32x32x16_bf16 v[80:95], %[k1], %[q1b], 0\n"
      "ds_read_b128 %[k1], %[a16] offset:7168\n"
      "v_exp_f32 v110, v70\n"
      "v_exp_f32 v111, v71\n"
      "v_cvt_pk_bf16_f32 v98, v108, v109\n"
      "v_cvt_pk_bf16_f32 v99, v110, v111\n"
      "v_exp_f32 v104, v72\n"
      "v_exp_f32 v105, v73\n"
      "v_mfma_f32_32x32x8_bf16 v[80:95], %[k2], %[q2b], v[80:95]\n"
      "ds_read_b64 %[k2], %[a8] offset:8192\n"
      "v_exp_f32 v106, v74\n"
      "v_exp_f32 v107, v75\n"
      "v_cvt_pk_bf16_f32 v100, v104, v105\n"
      "v_cvt_pk_bf16_f32 v101, v106, v107\n"
      "v_exp_f32 v108, v76\n"
      "v_exp_f32 v109, v77\n"
      "v_mfma_f32_32x32x16_bf16 %[oa], %[v0b], v[96:99], %[oa]\n"
      "v_exp_f32 v110, v78\n"
      "v_exp_f32 v111, v79\n"
      "v_cvt_pk_bf16_f32 v102, v108, v109\n"
      "v_cvt_pk_bf16_f32 v103, v110, v111\n"
      "v_exp_f32 v104, v80\n"
      "v_exp_f32 v105, v81\n"
      "s_waitcnt lgkmcnt(0)\n"
      "v_mfma_f32_32x32x16_bf16 %[oa], %[v1], v[100:103], %[oa]\n"
      "v_exp_f32 v106, v82\n"
      "v_exp_f32 v107, v83\n"
      "v_cvt_pk_bf16_f32 v96, v104, v105\n"
      "v_cvt_pk_bf16_f32 v97, v106, v107\n"
      "v_exp_f32 v108, v84\n"
      "v_exp_f32 v109, v85\n"
      "v_mfma_f32_32x32x16_bf16 v[64:79], %[k1], %[q1a], 0\n"
      "v_exp_f32 v110, v86\n"
      "v_exp_f32 v111, v87\n"
      "v_cvt_pk_bf16_f32 v98, v108, v109\n"
      "v_cvt_pk_bf16_f32 v99, v110, v111\n"
      "v_exp_f32 v104, v88\n"
      "v_exp_f32 v105, v89\n"
      "v_mfma_f32_32x32x8_bf16 v[64:79], %[k2], %[q2a], v[64:79]\n"
      "v_exp_f32 v106, v90\n"
      "v_exp_f32 v107, v91\n"
      "v_cvt_pk_bf16_f32 v100, v104, v105\n"
      "v_cvt_pk_bf16_f32 v101, v106, v107\n"
      "v_exp_f32 v108, v92\n"
      "v_exp_f32 v109, v93\n"
      "v_mfma_f32_32x32x16_bf16 %[ob], %[v0b], v[96:99], %[ob]\n"
      "v_exp_f32 v110, v94\n"
      "v_exp_f32 v111, v95\n"
      "v_cvt_pk_bf16_f32 v102, v108, v109\n"
      "v_cvt_pk_bf16_f32 v103, v110, v111\n"
      "v_exp_f32 v104, v64\n"
      "v_exp_f32 v105, v65\n"
      "v_mfma_f32_32x32x16_bf16 %[ob], %[v1], v[100:103], %[ob]\n"
      "ds_read_b128 %[v1], %[a16] offset:9728\n"
      "ds_read_b128 %[v0b], %[a16] offset:12288\n"
      "v_exp_f32 v106, v66\n"
      "v_exp_f32 v107, v67\n"
      "v_cvt_pk_bf16_f32 v96, v104, v105\n"
      "v_cvt_pk_bf16_f32 v97, v106, v107\n"
      "v_exp_f32 v108, v68\n"
      "v_exp_f32 v109, v69\n"
      "v_mfma_f32_32x32x16_bf16 v[80:95], %[k1], %[q1b], 0\n"
      "ds_read_b128 %[k1], %[a16] offset:10752\n"
      "v_exp_f32 v110, v70\n"
      "v_exp_f32 v111, v71\n"
      "v_cvt_pk_bf16_f32 v98, v108, v109\n"
      "v_cvt_pk_bf16_f32 v99, v110, v111\n"
      "v_exp_f32 v104, v72\n"
      "v_exp_f32 v105, v73\n"
      "v_mfma_f32_32x32x8_bf16 v[80:95], %[k2], %[q2b], v[80:95]\n"
      "ds_read_b64 %[k2], %[a8] offset:11776\n"
      "v_exp_f32 v106, v74\n"
      "v_exp_f32 v107, v75\n"
      "v_cvt_pk_bf16_f32 v100, v104, v105\n"
      "v_cvt_pk_bf16_f32 v101, v106, v107\n"
      "v_exp_f32 v108, v76\n"
      "v_exp_f32 v109, v77\n"
      "v_mfma_f32_32x32x16_bf16 %[oa], %[v0], v[96:99], %[oa]\n"
      "v_exp_f32 v110, v78\n"
      "v_exp_f32 v111, v79\n"
      "v_cvt_pk_bf16_f32 v102, v108, v109\n"
      "v_cvt_pk_bf16_f32 v103, v110, v111\n"
      "v_exp_f32 v104, v80\n"
      "v_exp_f32 v105, v81\n"
      "s_waitcnt lgkmcnt(0)\n"
      "v_mfma_f32_32x32x16_bf16 %[oa], %[v1], v[100:103], %[oa]\n"
      "v_exp_f32 v106, v82\n"
      "v_exp_f32 v107, v83\n"
      "v_cvt_pk_bf16_f32 v96, v104, v105\n"
      "v_cvt_pk_bf16_f32 v97, v106, v107\n"
      "v_exp_f32 v108, v84\n"
      "v_exp_f32 v109, v85\n"
      "v_mfma_f32_32x32x16_bf16 v[64:79], %[k1], %[q1a], 0\n"
      "v_exp_f32 v110, v86\n"
      "v_exp_f32 v111, v87\n"
      "v_cvt_pk_bf16_f32 v98, v108, v109\n"
      "v_cvt_pk_bf16_f32 v99, v110, v111\n"
      "v_exp_f32 v104, v88\n"
      "v_exp_f32 v105, v89\n"
      "v_mfma_f32_32x32x8_bf16 v[64:79], %[k2], %[q2a], v[64:79]\n"
      "v_exp_f32 v106, v90\n"
      "v_exp_f32 v107, v91\n"
      "v_cvt_pk_bf16_f32 v100, v104, v105\n"
      "v_cvt_pk_bf16_f32 v101, v106, v107\n"
      "v_exp_f32 v108, v92\n"
      "v_exp_f32 v109, v93\n"
      "v_mfma_f32_32x32x16_bf16 %[ob], %[v0], v[96:99], %[ob]\n"
      "v_exp_f32 v110, v94\n"
      "v_exp_f32 v111, v95\n"
      "v_cvt_pk_bf16_f32 v102, v108, v109\n"
      "v_cvt_pk_bf16_f32 v103, v110, v111\n"
      "v_exp_f32 v104, v64\n"
      "v_exp_f32 v105, v65\n"
      "v_mfma_f32_32x32x16_bf16 %[ob], %[v1], v[100:103], %[ob]\n"
      "ds_read_b128 %[v1], %[a16] offset:13312\n"
      "ds_read_b128 %[v0], %[a16] offset:15872\n"
      "v_exp_f32 v106, v66\n"
      "v_exp_f32 v107, v67\n"
      "v_cvt_pk_bf16_f32 v96, v104, v105\n"
      "v_cvt_pk_bf16_f32 v97, v106, v107\n"
      "v_exp_f32 v108, v68\n"
      "v_exp_f32 v109, v69\n"
      "v_mfma_f32_32x32x16_bf16 v[80:95], %[k1], %[q1b], 0\n"
      "ds_read_b128 %[k1], %[a16] offset:14336\n"
      "v_exp_f32 v110, v70\n"
      "v_exp_f32 v111, v71\n"
      "v_cvt_pk_bf16_f32 v98, v108, v109\n"
      "v_cvt_pk_bf16_f32 v99, v110, v111\n"
      "v_exp_f32 v104, v72\n"
      "v_exp_f32 v105, v73\n"
      "v_mfma_f32_32x32x8_bf16 v[80:95], %[k2], %[q2b], v[80:95]\n"
      "ds_read_b64 %[k2], %[a8] offset:15360\n"
      "v_exp_f32 v106, v74\n"
      "v_exp_f32 v107, v75\n"
      "v_cvt_pk_bf16_f32 v100, v104, v105\n"
      "v_cvt_pk_bf16_f32 v101, v106, v107\n"
      "v_exp_f32 v108, v76\n"
      "v_exp_f32 v109, v77\n"
      "v_mfma_f32_32x32x16_bf16 %[oa], %[v0b], v[96:99], %[oa]\n"
      "v_exp_f32 v110, v78\n"
      "v_exp_f32 v111, v79\n"
      "v_cvt_pk_bf16_f32 v102, v108, v109\n"
      "v_cvt_pk_bf16_f32 v103, v110, v111\n"
      "v_exp_f32 v104, v80\n"
      "v_exp_f32 v105, v81\n"
      "s_waitcnt lgkmcnt(0)\n"
      "v_mfma_f32_32x32x16_bf16 %[oa], %[v1], v[100:103], %[oa]\n"
      "v_exp_f32 v106, v82\n"
      "v_exp_f32 v107, v83\n"
      "v_cvt_pk_bf16_f32 v96, v104, v105\n"
      "v_cvt_pk_bf16_f32 v97, v106, v107\n"
      "v_exp_f32 v108, v84\n"
      "v_exp_f32 v109, v85\n"
      "v_mfma_f32_32x32x16_bf16 v[64:79], %[k1], %[q1a], 0\n"
      "v_exp_f32 v110, v86\n"
      "v_exp_f32 v111, v87\n"
      "v_cvt_pk_bf16_f32 v98, v108, v109\n"
      "v_cvt_pk_bf16_f32 v99, v110, v111\n"
      "v_exp_f32 v104, v88\n"
      "v_exp_f32 v105, v89\n"
      "v_mfma_f32_32x32x8_bf16 v[64:79], %[k2], %[q2a], v[64:79]\n"
      "v_exp_f32 v106, v90\n"
      "v_exp_f32 v107, v91\n"
      "v_cvt_pk_bf16_f32 v100, v104, v105\n"
      "v_cvt_pk_bf16_f32 v101, v106, v107\n"
      "v_exp_f32 v108, v92\n"
      "v_exp_f32 v109, v93\n"
      "v_mfma_f32_32x32x16_bf16 %[ob], %[v0b], v[96:99], %[ob]\n"
      "v_exp_f32 v110, v94\n"
      "v_exp_f32 v111, v95\n"
      "v_cvt_pk_bf16_f32 v102, v108, v109\n"
      "v_cvt_pk_bf16_f32 v103, v110, v111\n"
      "v_exp_f32 v104, v64\n"
      "v_exp_f32 v105, v65\n"
      "v_mfma_f32_32x32x16_bf16 %[ob], %[v1], v[100:103], %[ob]\n"
      "ds_read_b128 %[v1], %[a16] offset:16896\n"
      "ds_read_b128 %[v0b], %[a16] offset:19456\n"
      "v_exp_f32 v106, v66\n"
      "v_exp_f32 v107, v67\n"
      "v_cvt_pk_bf16_f32 v96, v104, v105\n"
      "v_cvt_pk_bf16_f32 v97, v106, v107\n"
      "v_exp_f32 v108, v68\n"
      "v_exp_f32 v109, v69\n"
      "v_mfma_f32_32x32x16_bf16 v[80:95], %[k1], %[q1b], 0\n"
      "ds_read_b128 %[k1], %[a16] offset:17920\n"
      "v_exp_f32 v110, v70\n"
      "v_exp_f32 v111, v71\n"
      "v_cvt_pk_bf16_f32 v98, v108, v109\n"
      "v_cvt_pk_bf16_f32 v99, v110, v111\n"
      "v_exp_f32 v104, v72\n"
      "v_exp_f32 v105, v73\n"
      "v_mfma_f32_32x32x8_bf16 v[80:95], %[k2], %[q2b], v[80:95]\n"
      "ds_read_b64 %[k2], %[a8] offset:18944\n"
      "v_exp_f32 v106, v74\n"
      "v_exp_f32 v107, v75\n"
      "v_cvt_pk_bf16_f32 v100, v104, v105\n"
      "v_cvt_pk_bf16_f32 v101, v106, v107\n"
      "v_exp_f32 v108, v76\n"
      "v_exp_f32 v109, v77\n"
      "v_mfma_f32_32x32x16_bf16 %[oa], %[v0], v[96:99], %[oa]\n"
      "v_exp_f32 v110, v78\n"
      "v_exp_f32 v111, v79\n"
      "v_cvt_pk_bf16_f32 v102, v108, v109\n"
      "v_cvt_pk_bf16_f32 v103, v110, v111\n"
      "v_exp_f32 v104, v80\n"
      "v_exp_f32 v105, v81\n"
      "s_waitcnt lgkmcnt(0)\n"
      "v_mfma_f32_32x32x16_bf16 %[oa], %[v1], v[100:103], %[oa]\n"
      "v_exp_f32 v106, v82\n"
      "v_exp_f32 v107, v83\n"
      "v_cvt_pk_bf16_f32 v96, v104, v105\n"
      "v_cvt_pk_bf16_f32 v97, v106, v107\n"
      "v_exp_f32 v108, v84\n"
      "v_exp_f32 v109, v85\n"
      "v_mfma_f32_32x32x16_bf16 v[64:79], %[k1], %[q1a], 0\n"
      "v_exp_f32 v110, v86\n"
      "v_exp_f32 v111, v87\n"
      "v_cvt_pk_bf16_f32 v98, v108, v109\n"
      "v_cvt_pk_bf16_f32 v99, v110, v111\n"
      "v_exp_f32 v104, v88\n"
      "v_exp_f32 v105, v89\n"
      "v_mfma_f32_32x32x8_bf16 v[64:79], %[k2], %[q2a], v[64:79]\n"
      "v_exp_f32 v106, v90\n"
      "v_exp_f32 v107, v91\n"
      "v_cvt_pk_bf16_f32 v100, v104, v105\n"
      "v_cvt_pk_bf16_f32 v101, v106, v107\n"
      "v_exp_f32 v108, v92\n"
      "v_exp_f32 v109, v93\n"
      "v_mfma_f32_32x32x16_bf16 %[ob], %[v0], v[96:99], %[ob]\n"
      "v_exp_f32 v110, v94\n"
      "v_exp_f32 v111, v95\n"
      "v_cvt_pk_bf16_f32 v102, v108, v109\n"
      "v_cvt_pk_bf16_f32 v103, v110, v111\n"
      "v_exp_f32 v104, v64\n"
      "v_exp_f32 v105, v65\n"
      "v_mfma_f32_32x32x16_bf16 %[ob], %[v1], v[100:103], %[ob]\n"
      "ds_read_b128 %[v1], %[a16] offset:20480\n"
      "ds_read_b128 %[v0], %[a16] offset:23040\n"
      "v_exp_f32 v106, v66\n"
      "v_exp_f32 v107, v67\n"
      "v_cvt_pk_bf16_f32 v96, v104, v105\n"
      "v_cvt_pk_bf16_f32 v97, v106, v107\n"
      "v_exp_f32 v108, v68\n"
      "v_exp_f32 v109, v69\n"
      "v_mfma_f32_32x32x16_bf16 v[80:95], %[k1], %[q1b], 0\n"
      "ds_read_b128 %[k1], %[a16] offset:21504\n"
      "v_exp_f32 v110, v70\n"
      "v_exp_f32 v111, v71\n"
      "v_cvt_pk_bf16_f32 v98, v108, v109\n"
      "v_cvt_pk_bf16_f32 v99, v110, v111\n"
      "v_exp_f32 v104, v72\n"
      "v_exp_f32 v105, v73\n"
      "v_mfma_f32_32x32x8_bf16 v[80:95], %[k2], %[q2b], v[80:95]\n"
      "ds_read_b64 %[k2], %[a8] offset:22528\n"
      "v_exp_f32 v106, v74\n"
      "v_exp_f32 v107, v75\n"
      "v_cvt_pk_bf16_f32 v100, v104, v105\n"
      "v_cvt_pk_bf16_f32 v101, v106, v107\n"
      "v_exp_f32 v108, v76\n"
      "v_exp_f32 v109, v77\n"
      "v_mfma_f32_32x32x16_bf16 %[oa], %[v0b], v[96:99], %[oa]\n"
      "v_exp_f32 v110, v78\n"
      "v_exp_f32 v111, v79\n"
      "v_cvt_pk_bf16_f32 v102, v108, v109\n"
      "v_cvt_pk_bf16_f32 v103, v110, v111\n"
      "v_exp_f32 v104, v80\n"
      "v_exp_f32 v105, v81\n"
      "s_waitcnt lgkmcnt(0)\n"
      "v_mfma_f32_32x32x16_bf16 %[oa], %[v1], v[100:103], %[oa]\n"
      "v_exp_f32 v106, v82\n"
      "v_exp_f32 v107, v83\n"
      "v_cvt_pk_bf16_f32 v96, v104, v105\n"
      "v_cvt_pk_bf16_f32 v97, v106, v107\n"
      "v_exp_f32 v108, v84\n"
      "v_exp_f32 v109, v85\n"
      "v_mfma_f32_32x32x16_bf16 v[64:79], %[k1], %[q1a], 0\n"
      "v_exp_f32 v110, v86\n"
      "v_exp_f32 v111, v87\n"
      "v_cvt_pk_bf16_f32 v98, v108, v109\n"
      "v_cvt_pk_bf16_f32 v99, v110, v111\n"
      "v_exp_f32 v104, v88\n"
      "v_exp_f32 v105, v89\n"
      "v_mfma_f32_32x32x8_bf16 v[64:79], %[k2], %[q2a], v[64:79]\n"
      "v_exp_f32 v106, v90\n"
      "v_exp_f32 v107, v91\n"
      "v_cvt_pk_bf16_f32 v100, v104, v105\n"
      "v_cvt_pk_bf16_f32 v101, v106, v107\n"
      "v_exp_f32 v108, v92\n"
      "v_exp_f32 v109, v93\n"
      "v_mfma_f32_32x32x16_bf16 %[ob], %[v0b], v[96:99], %[ob]\n"
      "v_exp_f32 v110, v94\n"
      "v_exp_f32 v111, v95\n"
      "v_cvt_pk_bf16_f32 v102, v108, v109\n"
      "v_cvt_pk_bf16_f32 v103, v110, v111\n"
      "v_exp_f32 v104, v64\n"
      "v_exp_f32 v105, v65\n"
      "v_mfma_f32_32x32x16_bf16 %[ob], %[v1], v[100:103], %[ob]\n"
      "ds_read_b128 %[v1], %[a16] offset:24064\n"
      "ds_read_b128 %[v0b], %[a16] offset:26624\n"
      "v_exp_f32 v106, v66\n"
      "v_exp_f32 v107, v67\n"
      "v_cvt_pk_bf16_f32 v96, v104, v105\n"
      "v_cvt_pk_bf16_f32 v97, v106, v107\n"
      "v_exp_f32 v108, v68\n"
      "v_exp_f32 v109, v69\n"
      "v_mfma_f32_32x32x16_bf16 v[80:95], %[k1], %[q1b], 0\n"
      "ds_read_b128 %[k1], %[a16] offset:25088\n"
      "v_exp_f32 v110, v70\n"
      "v_exp_f32 v111, v71\n"
      "v_cvt_pk_bf16_f32 v98, v108, v109\n"
      "v_cvt_pk_bf16_f32 v99, v110, v111\n"
      "v_exp_f32 v104, v72\n"
      "v_exp_f32 v105, v73\n"
      "v_mfma_f32_32x32x8_bf16 v[80:95], %[k2], %[q2b], v[80:95]\n"
      "ds_read_b64 %[k2], %[a8] offset:26112\n"
      "ds_read_b32 %[fv], %[fa]\n"
      "v_exp_f32 v106, v74\n"
      "v_exp_f32 v107, v75\n"
      "v_cvt_pk_bf16_f32 v100, v104, v105\n"
      "v_cvt_pk_bf16_f32 v101, v106, v107\n"
      "v_exp_f32 v108, v76\n"
      "v_exp_f32 v109, v77\n"
      "v_mfma_f32_32x32x16_bf16 %[oa], %[v0], v[96:99], %[oa]\n"
      "v_exp_f32 v110, v78\n"
      "v_exp_f32 v111, v79\n"
      "v_cvt_pk_bf16_f32 v102, v108, v109\n"
      "v_cvt_pk_bf16_f32 v103, v110, v111\n"
      "v_exp_f32 v104, v80\n"
      "v_exp_f32 v105, v81\n"
      "s_waitcnt lgkmcnt(0)\n"
      "v_mfma_f32_32x32x16_bf16 %[oa], %[v1], v[100:103], %[oa]\n"
      "v_exp_f32 v106, v82\n"
      "v_exp_f32 v107, v83\n"
      "v_cvt_pk_bf16_f32 v96, v104, v105\n"
      "v_cvt_pk_bf16_f32 v97, v106, v107\n"
      "v_exp_f32 v108, v84\n"
      "v_exp_f32 v109, v85\n"
      "v_mfma_f32_32x32x16_bf16 v[64:79], %[k1], %[q1a], 0\n"
      "v_exp_f32 v110, v86\n"
      "v_exp_f32 v111, v87\n"
      "v_cvt_pk_bf16_f32 v98, v108, v109\n"
      "v_cvt_pk_bf16_f32 v99, v110, v111\n"
      "v_exp_f32 v104, v88\n"
      "v_exp_f32 v105, v89\n"
      "v_mfma_f32_32x32x8_bf16 v[64:79], %[k2], %[q2a], v[64:79]\n"
      "v_exp_f32 v106, v90\n"
      "v_exp_f32 v107, v91\n"
      "v_cvt_pk_bf16_f32 v100, v104, v105\n"
      "v_cvt_pk_bf16_f32 v101, v106, v107\n"
      "v_exp_f32 v108, v92\n"
      "v_exp_f32 v109, v93\n"
      "v_mfma_f32_32x32x16_bf16 %[ob], %[v0], v[96:99], %[ob]\n"
      "v_exp_f32 v110, v94\n"
      "v_exp_f32 v111, v95\n"
      "v_cvt_pk_bf16_f32 v102, v108, v109\n"
      "v_cvt_pk_bf16_f32 v103, v110, v111\n"
      "s_cmp_eq_u32 %[st], 7\n"
      "s_cbranch_scc1 2f\n"
      "s_add_u32 %[tmp], %[st], 2\n"
      "v_readfirstlane_b32 %[t2], %[fv]\n"
      "s_cmp_ge_u32 %[t2], %[tmp]\n"
      "s_cbranch_scc1 2f\n"
      "3:\n"
      "ds_read_b32 %[fv], %[fa]\n"
      "s_waitcnt lgkmcnt(0)\n"
      "v_readfirstlane_b32 %[t2], %[fv]\n"
      "s_cmp_ge_u32 %[t2], %[tmp]\n"
      "s_cbranch_scc1 2f\n"
      "s_sleep 1\n"
      "s_branch 3b\n"
      "2:\n"
      "v_exp_f32 v104, v64\n"
      "v_exp_f32 v105, v65\n"
      "v_mfma_f32_32x32x16_bf16 %[ob], %[v1], v[100:103], %[ob]\n"
      "ds_read_b128 %[v1], %[a16] offset:27648\n"
      "ds_read_b128 %[v0], %[n16] offset:1536\n"
      "v_exp_f32 v106, v66\n"
      "v_exp_f32 v107, v67\n"
      "v_cvt_pk_bf16_f32 v96, v104, v105\n"
      "v_cvt_pk_bf16_f32 v97, v106, v107\n"
      "v_exp_f32 v108, v68\n"
      "v_exp_f32 v109, v69\n"
      "v_mfma_f32_32x32x16_bf16 v[80:95], %[k1], %[q1b], 0\n"
      "ds_read_b128 %[k1], %[n16]\n"
      "v_exp_f32 v110, v70\n"
      "v_exp_f32 v111, v71\n"
      "v_cvt_pk_bf16_f32 v98, v108, v109\n"
      "v_cvt_pk_bf16_f32 v99, v110, v111\n"
      "v_exp_f32 v104, v72\n"
      "v_exp_f32 v105, v73\n"
      "v_mfma_f32_32x32x8_bf16 v[80:95], %[k2], %[q2b], v[80:95]\n"
      "ds_read_b64 %[k2], %[n8] offset:1024\n"
      "v_exp_f32 v106, v74\n"
      "v_exp_f32 v107, v75\n"
      "v_cvt_pk_bf16_f32 v100, v104, v105\n"
      "v_cvt_pk_bf16_f32 v101, v106, v107\n"
      "v_exp_f32 v108, v76\n"
      "v_exp_f32 v109, v77\n"
      "v_mfma_f32_32x32x16_bf16 %[oa], %[v0b], v[96:99], %[oa]\n"
      "v_exp_f32 v110, v78\n"
      "v_exp_f32 v111, v79\n"
      "v_cvt_pk_bf16_f32 v102, v108, v109\n"
      "v_cvt_pk_bf16_f32 v103, v110, v111\n"
      "v_exp_f32 v104, v80\n"
      "v_exp_f32 v105, v81\n"
      "s_waitcnt lgkmcnt(0)\n"
      "v_mfma_f32_32x32x16_bf16 %[oa], %[v1], v[100:103], %[oa]\n"
      "v_exp_f32 v106, v82\n"
      "v_exp_f32 v107, v83\n"
      "v_cvt_pk_bf16_f32 v96, v104, v105\n"
      "v_cvt_pk_bf16_f32 v97, v106, v107\n"
      "v_exp_f32 v108, v84\n"
      "v_exp_f32 v109, v85\n"
      "v_mfma_f32_32x32x16_bf16 v[64:79], %[k1], %[q1a], 0\n"
      "v_exp_f32 v110, v86\n"
      "v_exp_f32 v111, v87\n"
      "v_cvt_pk_bf16_f32 v98, v108, v109\n"
      "v_cvt_pk_bf16_f32 v99, v110, v111\n"
      "v_exp_f32 v104, v88\n"
      "v_exp_f32 v105, v89\n"
      "v_mfma_f32_32x32x8_bf16 v[64:79], %[k2], %[q2a], v[64:79]\n"
      "v_exp_f32 v106, v90\n"
      "v_exp_f32 v107, v91\n"
      "v_cvt_pk_bf16_f32 v100, v104, v105\n"
      "v_cvt_pk_bf16_f32 v101, v106, v107\n"
      "v_exp_f32 v108, v92\n"
      "v_exp_f32 v109, v93\n"
      "v_mfma_f32_32x32x16_bf16 %[ob], %[v0b], v[96:99], %[ob]\n"
      "v_exp_f32 v110, v94\n"
      "v_exp_f32 v111, v95\n"
      "v_cvt_pk_bf16_f32 v102, v108, v109\n"
      "v_cvt_pk_bf16_f32 v103, v110, v111\n"
      "s_add_u32 %[st], %[st], 1\n"
      "v_mov_b32 %[fv], %[st]\n"
      "ds_write_b32 %[da], %[fv]\n"
      "s_mov_b32 %[tmp], %[b0]\n"
      "s_mov_b32 %[b0], %[b1]\n"
      "s_mov_b32 %[b1], %[b2]\n"
      "s_mov_b32 %[b2], %[tmp]\n"
      "s_cmp_lt_u32 %[st], 8\n"
      "s_cbranch_scc1 1b\n"
      "s_nop 1\n"
      "v_mfma_f32_32x32x16_bf16 %[ob], %[v1], v[100:103], %[ob]\n"
      "s_waitcnt lgkmcnt(0)\n"
      "s_nop 15\n"
      : [oa] "+v"(oa), [ob] "+v"(ob), [k1] "=&v"(k1), [k2] "=&v"(k2), [v0] "=&v"(v0), [v0b] "=&v"(v0b), [v1] "+v"(v1),
        [a16] "=&v"(a16), [a8] "=&v"(a8), [n16] "=&v"(n16), [n8] "=&v"(n8),
        [b0] "+s"(b0), [b1] "+s"(b1), [b2] "+s"(b2), [st] "=&s"(st), [tmp] "=&s"(tmp), [t2] "=&s"(t2), [fv] "=&v"(fv)
      : [q1a] "v"(q1a), [q2a] "v"(q2a), [q1b] "v"(q1b), [q2b] "v"(q2b), [base16] "v"(base16), [base8] "v"(base8),
        [fa] "v"(fa), [da] "v"(da)
      : "memory", "scc", "v64", "v65", "v66", "v67", "v68", "v69", "v70", "v71", "v72", "v73", "v74", "v75", "v76", "v77", "v78", "v79", "v80", "v81", "v82", "v83", "v84", "v85", "v86", "v87", "v88", "v89", "v90", "v91", "v92", "v93", "v94", "v95", "v96", "v97", "v98", "v99", "v100", "v101", "v102", "v103", "v104", "v105", "v106", "v107", "v108", "v109", "v110", "v111");
  }
  const int h = lane & 31, hf = lane >> 5;
  const int s0 = sb + pr * 2;
  char* img = lds + K2_QI + pr * 2560;
#pragma unroll
  for (int a = 0; a < 2; ++a) {
    const f32x16& o = a ? ob : oa;
    const int s = s0 + a;
    const float l = x32_sum(hf ? o[8] : 0.f);
    char* orow = img + a * 1280 + h * 40;
    const bool bad = !(l >= 8.2718061e-25f) || !(l <= 3.0e38f);
    if (__builtin_expect(__any(bad), 0)) {
      const char* qb = ws + WS_QP + (size_t)(s >> 5) * 80 * 512 + (s & 31) * 16;
      float q[20], oo[20];
#pragma unroll
      for (int d = 0; d < 20; ++d) { const int col = 20 * h + d; q[d] = bf2f(*(const __bf16*)(qb + (col >> 3) * 512 + (col & 7) * 2)); oo[d] = 0.f; }
      float m = -3.0e38f, ls = 0.f;
#pragma unroll 1
      for (int kk = 0; kk < S_; ++kk) {
        const char* tb = KVf + (size_t)(kk >> 5) * KV_TILE;
        const int kl = kk & 31;
        float sc = 0.f;
#pragma unroll
        for (int d = 0; d < 16; ++d) sc += q[d] * bf2f(*(const __bf16*)(tb + (kl + 32 * (d >> 3)) * 16 + (d & 7) * 2));
#pragma unroll
        for (int d = 16; d < 20; ++d) sc += q[d] * bf2f(*(const __bf16*)(tb + 1024 + kl * 8 + (d - 16) * 2));
        const float mn = fmaxf(m, sc), al = __builtin_amdgcn_exp2f(m - mn), p = __builtin_amdgcn_exp2f(sc - mn);
        const int sx = kl >> 4, k16 = kl & 15, vh = (k16 >> 2) & 1, jj = (k16 & 3) + 4 * (k16 >> 3);
        ls = ls * al + p;
#pragma unroll
        for (int d = 0; d < 20; ++d) oo[d] = oo[d] * al + p * bf2f(*(const __bf16*)(tb + 1536 + sx * 1024 + (d + 32 * vh) * 16 + jj * 2));
        m = mn;
      }
      const float inv = 1.f / ls;
      if (hf == 0) {
#pragma unroll
        for (int d4 = 0; d4 < 5; ++d4) *(bf16x4*)(orow + d4 * 8) = pack4(oo[4 * d4] * inv, oo[4 * d4 + 1] * inv, oo[4 * d4 + 2] * inv, oo[4 * d4 + 3] * inv);
      }
    } else {
      const float inv = __builtin_amdgcn_rcpf(l);
      *(bf16x4*)(orow + hf * 8) = pack4(o[0] * inv, o[1] * inv, o[2] * inv, o[3] * inv);
      *(bf16x4*)(orow + 16 + hf * 8) = pack4(o[4] * inv, o[5] * inv, o[6] * inv, o[7] * inv);
      if (hf == 0) *(bf16x4*)(orow + 32) = pack4(o[8] * inv, o[9] * inv, o[10] * inv, o[11] * inv);
    }
  }
  asm volatile("s_waitcnt lgkmcnt(0)" ::: "memory");
  {
    char* op = ws + WS_OB + (size_t)s0 * 1280 + lane * 16;
    st16(op, *(const u32x4*)(img + lane * 16));
    st16(op + 1024, *(const u32x4*)(img + 1024 + lane * 16));
    if (lane < 32) st16(op + 2048, *(const u32x4*)(img + 2048 + lane * 16));
  }
}

constexpr int K3_XB = 24576;

__global__ __launch_bounds__(512, 2) void k3_out(const __bf16* __restrict__ woA, const __bf16* __restrict__ woB,
                                                 char* __restrict__ ws, __bf16* __restrict__ out) {
  __shared__ __attribute__((aligned(16))) char lds[G_NB * G_STAGE];
  const int tid = threadIdx.x, lane = tid & 63, w = __builtin_amdgcn_readfirstlane(tid >> 6);
  const int T = (blockIdx.x & 7) * 28 + (blockIdx.x >> 3);
  if (T >= 220) return;
  const int rt = (T * 205) >> 11, ct = T - rt * 10;
  const unsigned lbase = (unsigned)(size_t)lds;
  const int l15 = lane & 15, g = lane >> 4;
  if (__builtin_expect(w >= 4, 1)) {
    const int lw = w - 4;
    const unsigned wv0 = (lane >> 3) * 1280 + ((((lane & 7) >> 1) ^ ((lane >> 4) & 1)) << 5) + ((lane & 1) << 4), wv1 = wv0 ^ 64;
    unsigned aoff[6], al[6];
#pragma unroll
    for (int ii = 0; ii < 6; ++ii) {
      int p = lw * 6 + ii + ct; p = p >= 24 ? p - 24 : p;
      const int row = 4 * p + (lane >> 4);
      int s = rt * 96 + row; s = s > S_ - 1 ? S_ - 1 : s;
      aoff[ii] = s * 1280 + (((lane & 15) ^ (row & 15)) << 4);
      al[ii] = p * 1024;
    }
    auto issue = [&](int st, int buf) {
      const int part = st >= 5, kk = st - 5 * part;
      const char* ab = ws + (part ? WS_OB : WS_OA) + kk * 256;
      const char* wp = (const char*)(part ? woB : woA) + (size_t)(kk * 128 + lw * 32) * 1280 + ct * 128;
      const unsigned lb = lbase + buf * G_STAGE;
      const unsigned wl = lb + K3_XB + lw * 4096;
      dma16(ab, aoff[0], lb + al[0]); dma16(wp, wv0, wl); dma16(ab, aoff[1], lb + al[1]);
      dma16(ab, aoff[2], lb + al[2]); dma16(wp + 8 * 1280, wv1, wl + 1024); dma16(ab, aoff[3], lb + al[3]);
      dma16(wp + 16 * 1280, wv0, wl + 2048); dma16(ab, aoff[4], lb + al[4]); dma16(ab, aoff[5], lb + al[5]);
      dma16(wp + 24 * 1280, wv1, wl + 3072);
    };
    issue(0, 0); issue(1, 1);
    WAIT_BAR(10);
    issue(2, 2);
    WAIT_BAR(10);
#pragma unroll 1
    for (int st = 0; st < 9; ++st) {
      if (st < 7) { issue(st + 3, (st + 3) & 3); WAIT_BAR(10); }
      else { WAIT_BAR(0); }
    }
  } else {
    const int wm = w >> 1, wn = w & 1;
    const int p4 = lane & 3, q4 = l15 >> 2;
    const int xo0 = (wm * 48 + l15) * 256 + ((g ^ l15) << 4);
    const int f = ((q4 >> 1) & 1) | ((g & 1) << 1);
    int wb[2];
#pragma unroll
    for (int j = 0; j < 2; ++j) wb[j] = K3_XB + (8 * g + q4) * 128 + 8 * p4 + 32 * ((2 * wn + j) ^ f);
    float gv[3];
#pragma unroll
    for (int mi = 0; mi < 3; ++mi) {
      int s = rt * 96 + wm * 48 + mi * 16 + l15; s = s > S_ - 1 ? S_ - 1 : s;
      const float gg = ((const float*)(ws + WS_G))[s];
      gv[mi] = gg;
    }
    f32x4 acc[3][2] = {}, accA[3][2];
    bf16x8 xf[4][3], wf[4][2];
    auto ldf = [&](int set, const char* sb, int ks) {
#pragma unroll
      for (int mi = 0; mi < 3; ++mi) xf[set][mi] = *(const bf16x8*)(sb + (xo0 ^ (ks * 64)) + mi * 4096);
#pragma unroll
      for (int ni = 0; ni < 2; ++ni) {
        const char* pw = sb + wb[ni] + ks * 32 * 128;
        wf[set][ni] = cat8(trread(pw), trread(pw + 4 * 128));
      }
    };
    auto mm = [&](int set) {
#pragma unroll
      for (int mi = 0; mi < 3; ++mi)
#pragma unroll
        for (int ni = 0; ni < 2; ++ni) acc[mi][ni] = mfma16(wf[set][ni], xf[set][mi], acc[mi][ni]);
#pragma unroll
      for (int i = 0; i < 6; ++i) { __builtin_amdgcn_sched_group_barrier(0x8, 1, 0); __builtin_amdgcn_sched_group_barrier(0x100, 1, 0); }
      __builtin_amdgcn_sched_group_barrier(0x100, 1, 0);
      __builtin_amdgcn_sched_barrier(0);
    };
    WAIT_BAR(10);
    ldf(0, lds, 0); ldf(1, lds, 1);
#pragma unroll
    for (int st = 0; st < 10; ++st) {
      const char* sb = lds + (st & 3) * G_STAGE;
      const char* sn = lds + ((st + 1) & 3) * G_STAGE;
      if (st == 5) {
#pragma unroll
        for (int mi = 0; mi < 3; ++mi)
#pragma unroll
          for (int ni = 0; ni < 2; ++ni) { accA[mi][ni] = acc[mi][ni] * gv[mi]; acc[mi][ni] = f32x4{0.f, 0.f, 0.f, 0.f}; }
      }
      ldf(2, sb, 2); mm(0);
      ldf(3, sb, 3); mm(1);
      if (st == 0) RAWBAR();
      if (st < 9) ldf(0, sn, 0);
      mm(2);
      if (st < 9) ldf(1, sn, 1);
      mm(3);
      if (st < 9) RAWBAR();
    }
#pragma unroll
    for (int mi = 0; mi < 3; ++mi) {
      const u32x4 v = pair_pack(accA[mi][0] + acc[mi][0] * (1.f - gv[mi]), accA[mi][1] + acc[mi][1] * (1.f - gv[mi]));
      *(u32x4*)(lds + (wm * 48 + mi * 16 + l15) * 144 + wn * 64 + (g & 1) * 32 + (g >> 1) * 16) = v;
    }
    BAR();
#pragma unroll
    for (int j = 0; j < 3; ++j) {
      const int row = w * 24 + j * 8 + (lane >> 3), s = rt * 96 + row;
      if (s < S_) st16((char*)out + (size_t)s * 1280 + ct * 128 + (lane & 7) * 16, *(const u32x4*)(lds + row * 144 + (lane & 7) * 16));
    }
  }
}

extern "C" void kernel_launch(void* const* d_in, const int* in_sizes, int n_in, void* d_out, int out_size, void* d_ws,
                              size_t ws_size, hipStream_t stream) {
  const __bf16* x = (const __bf16*)d_in[0];
  const __bf16* wqkv = (const __bf16*)d_in[1];
  const __bf16* woA = (const __bf16*)d_in[2];
  const __bf16* wq = (const __bf16*)d_in[4];
  const __bf16* wk = (const __bf16*)d_in[5];
  const __bf16* wv = (const __bf16*)d_in[6];
  const __bf16* woB = (const __bf16*)d_in[7];
  const __bf16* gw = (const __bf16*)d_in[8];
  char* ws = (char*)d_ws;
  k1_proj<<<256, 768, 0, stream>>>(x, wqkv, wq, wk, wv, gw, ws);
  k2_attn<<<256, 576, 0, stream>>>(ws, (const char*)woA, (const char*)woB);
  k3_out<<<224, 512, 0, stream>>>(woA, woB, ws, (__bf16*)d_out);
}
```
